```python
import torch

def _softmax(x, axis=-1):
    return torch.softmax(x, dim=axis)
def _gelu(x):
    return torch.nn.functional.gelu(x, approximate="tanh")
def _silu(x):
    return torch.nn.functional.silu(x)
def _sigmoid(x):
    return torch.sigmoid(x)
def _layernorm(x, axis=-1, eps=1e-5):
    m = x.mean(dim=axis, keepdim=True)
    v = x.var(dim=axis, keepdim=True, unbiased=False)
    return (x - m) / torch.sqrt(v + eps)
def _scatter_add(tgt, idx, src, axis=0):
    return tgt.clone().index_add_(axis, idx, src)
def _segment_sum(src, seg, n):
    out = torch.zeros((n,) + src.shape[1:], dtype=src.dtype)
    return out.index_add_(0, seg, src)
def _pad(x, pw):
    flat = tuple(v for pair in reversed(pw) for v in pair)
    return torch.nn.functional.pad(x, flat)

kernel_name = 'parallel_sum__sliding_block_cached__mixer_channel_mlp__decode'

def setup_inputs():

    _w = (lambda a: (lambda t: t.to(torch.bfloat16) if t.is_floating_point() else t)(torch.as_tensor(a)))
    def _A_setup():
        B, Sq, D = 1, 1, 448
        Hq, Hkv, Sk, Dh = 16, 8, 32768, 256
        assert Hq % Hkv == 0, (Hq, Hkv)
        return {
            "x": _w((lambda *s: torch.randn(*s, dtype=torch.bfloat16) * 0.05)(B, Sq, D)),
            "k_cache": _w((lambda *s: torch.randn(*s, dtype=torch.bfloat16) * 3.2)(B, Hkv, Sk, Dh)),
            "v_cache": _w((lambda *s: torch.randn(*s, dtype=torch.bfloat16) * 0.4)(B, Hkv, Sk, Dh)),
            "wq": _w((lambda *s: torch.randn(*s, dtype=torch.bfloat16) * 3.2)(D, Hq * Dh) * (D ** -0.5)),
            "wo": _w((lambda *s: torch.randn(*s, dtype=torch.bfloat16) * 0.4)(Hq * Dh, D) * ((Hq * Dh) ** -0.5)),
        }
    _A_in = _A_setup()

    def _B_setup():
        B, S, D, F = 1, 1, 448, 1792
        return {
            "x": _w((lambda *s: torch.randn(*s, dtype=torch.bfloat16) * 0.05)(B, S, D)),
            "w1": _w((lambda *s: torch.randn(*s, dtype=torch.bfloat16) * 3.2)(D, F) * (D ** -0.5)),
            "b1": _w((lambda *s: torch.randn(*s, dtype=torch.bfloat16) * 1.6)(F)),
            "w2": _w((lambda *s: torch.randn(*s, dtype=torch.bfloat16) * 0.1)(F, D) * (F ** -0.5)),
            "b2": _w((lambda *s: torch.randn(*s, dtype=torch.bfloat16) * 0.1)(D)),
        }
    _B_in = _B_setup()

    return {
        "A_x": _A_in["x"],
        "A_k_cache": _A_in["k_cache"],
        "A_v_cache": _A_in["v_cache"],
        "A_wq": _A_in["wq"],
        "A_wo": _A_in["wo"],
        "B_x": _B_in["x"],
        "B_w1": _B_in["w1"],
        "B_b1": _B_in["b1"],
        "B_w2": _B_in["w2"],
        "B_b2": _B_in["b2"],
    }

def reference(A_x, A_k_cache, A_v_cache, A_wq, A_wo, B_x, B_w1, B_b1, B_w2, B_b2):
    def _A_ref(x, k_cache, v_cache, wq, wo):
        B, Sq, D = x.shape
        Hq, Dh = 16, 256
        Hkv, Sk = k_cache.shape[1], k_cache.shape[2]
        W = min(4096, max(256, Sk // 4))
        rep = Hq // Hkv
        q = (lambda x, *d: x.permute(*d))((x @ wq).reshape(B, Sq, Hq, Dh), 0, 2, 1, 3)
        k = (lambda x, n, axis: torch.repeat_interleave(x, n, dim=axis))(k_cache[:, :, Sk - W:, :], rep, axis=1)
        v = (lambda x, n, axis: torch.repeat_interleave(x, n, dim=axis))(v_cache[:, :, Sk - W:, :], rep, axis=1)
        scores = torch.einsum("bhqd,bhkd->bhqk", q, k) * (Dh ** -0.5)
        p = _softmax(scores, axis=-1)
        o = torch.einsum("bhqk,bhkd->bhqd", p, v)
        return (lambda x, *d: x.permute(*d))(o, 0, 2, 1, 3).reshape(B, Sq, Hq * Dh) @ wo

    def _B_ref(x, w1, b1, w2, b2):
        y = _layernorm(x, axis=-1)
        h = _gelu(torch.einsum("bsd,df->bsf", y, w1) + b1)
        return torch.einsum("bsf,fd->bsd", h, w2) + b2

    x = A_x
    xn = _layernorm(x, axis=-1)
    return x + _A_ref(xn, A_k_cache, A_v_cache, A_wq, A_wo) + _B_ref(xn, B_w1, B_b1, B_w2, B_b2)
```

```cpp
#include <hip/hip_runtime.h>
#include <hip/hip_bf16.h>
#include <stdint.h>

#define GAS __attribute__((address_space(1)))
typedef unsigned u32x4 __attribute__((ext_vector_type(4)));
typedef float f32x4 __attribute__((ext_vector_type(4)));
typedef __bf16 bf16x8 __attribute__((ext_vector_type(8)));
typedef __bf16 bf16x2 __attribute__((ext_vector_type(2)));
typedef unsigned short u16;
typedef unsigned long long u64;
typedef GAS u64 gu64;
#define RLX_AGENT __ATOMIC_RELAXED, __HIP_MEMORY_SCOPE_AGENT

namespace {

constexpr int D = 448, HKV = 8, SK = 32768, DH = 256, WIN = 4096, FF = 1792;
constexpr int NQ = 4096;
constexpr int NWG = 256, NTHR = 512;
constexpr int NCONS = 112;
constexpr float EPS = 1e-5f;
constexpr float SCALE_LOG2E = 0.0625f * 1.4426950408889634f;
constexpr unsigned SENT = 0xFFFFFFFFu;
constexpr unsigned SPIN_CAP = 200000u;

constexpr unsigned WS_E = 0;
constexpr unsigned WS_F = 4096;
constexpr unsigned WS_QG = 8192;
constexpr unsigned WS_S2S = 65536;
constexpr unsigned WS_S2D = 131072;
constexpr unsigned WS_S3 = 1048576;
constexpr unsigned WS_BYTES = WS_S3 + NCONS * 4096;

__device__ __forceinline__ float bf_lo(unsigned v) { return __builtin_bit_cast(float, v << 16); }
__device__ __forceinline__ float bf_hi(unsigned v) { return __builtin_bit_cast(float, v & 0xffff0000u); }
__device__ __forceinline__ float bf2f(u16 v) { return __builtin_bit_cast(float, (unsigned)v << 16); }
__device__ __forceinline__ unsigned cvtpk(float lo, float hi) {
  const bf16x2 v = {(__bf16)lo, (__bf16)hi};
  return __builtin_bit_cast(unsigned, v);
}
__device__ __forceinline__ float rbf(float x) { return bf_lo(cvtpk(x, x)); }

template <int CTRL> __device__ __forceinline__ float dpp(float x) {
  return __builtin_bit_cast(float, __builtin_amdgcn_mov_dpp(__builtin_bit_cast(int, x), CTRL, 0xf, 0xf, true));
}
constexpr int ROR1 = 0x121, ROR2 = 0x122, ROR4 = 0x124, ROR8 = 0x128;
__device__ __forceinline__ float row_sum(float x) {
  x += dpp<ROR8>(x); x += dpp<ROR4>(x); x += dpp<ROR2>(x); x += dpp<ROR1>(x); return x;
}
__device__ __forceinline__ float row_max(float x) {
  x = fmaxf(x, dpp<ROR8>(x)); x = fmaxf(x, dpp<ROR4>(x)); x = fmaxf(x, dpp<ROR2>(x)); x = fmaxf(x, dpp<ROR1>(x)); return x;
}
__device__ __forceinline__ float fold32(float a, float b) {
  auto r = __builtin_amdgcn_permlane32_swap(__builtin_bit_cast(unsigned, a), __builtin_bit_cast(unsigned, b), false, false);
  return __uint_as_float(r[0]) + __uint_as_float(r[1]);
}
__device__ __forceinline__ float fold16(float a, float b) {
  auto r = __builtin_amdgcn_permlane16_swap(__builtin_bit_cast(unsigned, a), __builtin_bit_cast(unsigned, b), false, false);
  return __uint_as_float(r[0]) + __uint_as_float(r[1]);
}
__device__ __forceinline__ float xrow_sum(float x) { x = fold16(x, x); return fold32(x, x); }
__device__ __forceinline__ float xrow_max(float x) {
  auto s = __builtin_amdgcn_permlane16_swap(__builtin_bit_cast(unsigned, x), __builtin_bit_cast(unsigned, x), false, false);
  x = fmaxf(__uint_as_float(s[0]), __uint_as_float(s[1]));
  auto t = __builtin_amdgcn_permlane32_swap(__builtin_bit_cast(unsigned, x), __builtin_bit_cast(unsigned, x), false, false);
  return fmaxf(__uint_as_float(t[0]), __uint_as_float(t[1]));
}
__device__ __forceinline__ float wave_sum(float x) { return xrow_sum(row_sum(x)); }

__device__ __forceinline__ u32x4 ld16(const u16* p) { return *reinterpret_cast<const u32x4*>(p); }
__device__ __forceinline__ u32x4 ld16nt(const u16* p) { return __builtin_nontemporal_load(reinterpret_cast<const u32x4*>(p)); }
__device__ __forceinline__ f32x4 mfma(u32x4 a, u32x4 b, f32x4 c) {
  return __builtin_amdgcn_mfma_f32_16x16x32_bf16(__builtin_bit_cast(bf16x8, a), __builtin_bit_cast(bf16x8, b), c, 0, 0, 0);
}
__device__ __forceinline__ unsigned san(float v) {
  unsigned b = __builtin_bit_cast(unsigned, v);
  return b == SENT ? 0x7fc00000u : b;
}
__device__ __forceinline__ u32x4 san4(f32x4 v) { return u32x4{san(v[0]), san(v[1]), san(v[2]), san(v[3])}; }
__device__ __forceinline__ unsigned umax4(u32x4 v) { return max(max(v[0], v[1]), max(v[2], v[3])); }
__device__ __forceinline__ float asf(unsigned v) { return __builtin_bit_cast(float, v); }

struct Smem {
  float obuf[8][512];
  float stats[8][4];
  float qpart[8][16];
  float hpart[8][8];
  u16 qs[512];
};

__global__ void __launch_bounds__(NTHR, 2)
fused_kernel(const u16* __restrict__ x, const u16* __restrict__ kc, const u16* __restrict__ vc,
             const u16* __restrict__ wq, const u16* __restrict__ wo, const u16* __restrict__ w1,
             const u16* __restrict__ b1, const u16* __restrict__ w2, const u16* __restrict__ b2,
             u16* __restrict__ out, unsigned char* ws) {
  __shared__ __attribute__((aligned(16))) Smem sm;
  const int tid = threadIdx.x, lane = tid & 63;
  const int wave = __builtin_amdgcn_readfirstlane(tid >> 6);
  const int b = blockIdx.x, h = b & 7, s = b >> 3;
  const int g = lane >> 4, n = lane & 15;
  const __amdgpu_buffer_rsrc_t rs = __builtin_amdgcn_make_buffer_rsrc(ws, 0, WS_BYTES, 0x00020000);

  {
    const u32x4 sent4 = {SENT, SENT, SENT, SENT};
    if (tid < 128) __builtin_amdgcn_raw_buffer_store_b128(sent4, rs, WS_S2D + (unsigned)(h * 32 + s) * 2048u + tid * 16u, 0, 16);
    else if (tid == 128) __builtin_amdgcn_raw_buffer_store_b128(sent4, rs, WS_S2S + (unsigned)(h * 32 + s) * 16u, 0, 16);
    else if (tid >= 256 && b < NCONS) __builtin_amdgcn_raw_buffer_store_b128(sent4, rs, WS_S3 + (unsigned)b * 4096u + (tid - 256) * 16u, 0, 16);
  }
  asm volatile("" ::: "memory");
  const u64 epoch = __hip_atomic_load((gu64*)(ws + WS_E), RLX_AGENT);
  const u32x4 xv = ld16(x + 8 * (lane < 56 ? lane : 0));
  const int r0 = tid >> 1, r1 = 256 + (tid >> 1);
  const bool has1 = wave < 6;
  const u16 xr0 = x[r0];
  const u16 xr1 = x[has1 ? r1 : 0];
  const u16 xrt = x[tid < D ? tid : 0];
  const u16* wqp = wq + h * 512 + s * 16 + (tid & 1) * 8;
  const u32x4 wq0 = ld16(wqp + (size_t)r0 * NQ);
  u32x4 wq1;
  if (has1) wq1 = ld16(wqp + (size_t)r1 * NQ);
  __builtin_amdgcn_sched_barrier(0);
  const size_t kvbase = ((size_t)h * SK + (SK - WIN) + s * 128 + wave * 16) * DH;
  u32x4 kreg[8];
  {
    const u16* kp = kc + kvbase + (size_t)n * DH + g * 8;
#pragma unroll
    for (int st = 0; st < 8; ++st) kreg[st] = ld16nt(kp + st * 32);
  }
  __builtin_amdgcn_sched_barrier(0);

  float xf[8];
#pragma unroll
  for (int i = 0; i < 4; ++i) { xf[2 * i] = bf_lo(xv[i]); xf[2 * i + 1] = bf_hi(xv[i]); }
  float sum = 0.f;
#pragma unroll
  for (int i = 0; i < 8; ++i) sum += xf[i];
  if (lane >= 56) sum = 0.f;
  const float mean = wave_sum(sum) * (1.f / D);
  float sq = 0.f;
#pragma unroll
  for (int i = 0; i < 8; ++i) { const float d = xf[i] - mean; sq += d * d; }
  if (lane >= 56) sq = 0.f;
  const float rstd = 1.f / sqrtf(wave_sum(sq) * (1.f / D) + EPS);
  const float xn0 = rbf((bf2f(xr0) - mean) * rstd);
  const float xn1 = rbf((bf2f(xr1) - mean) * rstd);

  {
    float a[8];
#pragma unroll
    for (int i = 0; i < 4; ++i) { a[2 * i] = xn0 * bf_lo(wq0[i]); a[2 * i + 1] = xn0 * bf_hi(wq0[i]); }
    if (has1) {
#pragma unroll
      for (int i = 0; i < 4; ++i) { a[2 * i] += xn1 * bf_lo(wq1[i]); a[2 * i + 1] += xn1 * bf_hi(wq1[i]); }
    }
    float c4[4], c2[2];
#pragma unroll
    for (int i = 0; i < 4; ++i) c4[i] = fold32(a[i], a[i + 4]);
#pragma unroll
    for (int i = 0; i < 2; ++i) c2[i] = fold16(c4[i], c4[i + 2]);
#pragma unroll
    for (int i = 0; i < 2; ++i) { float t = c2[i]; t += dpp<ROR8>(t); t += dpp<ROR4>(t); t += dpp<ROR2>(t); c2[i] = t; }
    if (n < 2) {
      const int col = n * 8 + 2 * (g & 1) + 4 * (g >> 1);
      sm.qpart[wave][col] = c2[0]; sm.qpart[wave][col + 1] = c2[1];
    }
  }
  asm volatile("s_waitcnt vmcnt(8)" ::: "memory");
  __syncthreads();
  const u64 tagw = (epoch + 1ull) << 16;
  if (wave == 0) {
    if (lane < 16) {
      float qv = 0.f;
#pragma unroll
      for (int w = 0; w < 8; ++w) qv += sm.qpart[w][lane];
      const u64 gr = tagw | (u64)(cvtpk(qv, qv) & 0xffffu);
      __hip_atomic_store((gu64*)(ws + WS_QG) + (h * 512 + s * 16 + lane), gr, RLX_AGENT);
    } else if (lane == 16) {
      __hip_atomic_store((gu64*)(ws + WS_F) + b, epoch + 1ull, RLX_AGENT);
    }
  }
  asm volatile("" ::: "memory");
  __builtin_amdgcn_sched_barrier(0);

  u32x4 vreg[4][2];
  {
    const u16* vp = vc + kvbase + (size_t)(4 * g) * DH + n * 8;
#pragma unroll
    for (int t = 0; t < 4; ++t)
#pragma unroll
      for (int hf = 0; hf < 2; ++hf) vreg[t][hf] = ld16nt(vp + t * DH + hf * 128);
  }
  __builtin_amdgcn_sched_barrier(0);
  const bool has_mlp = s < 28;
  const int fb = h * 28 + s;
  const bool do_w1 = has_mlp && wave < 7;
  u32x4 w1v;
  if (do_w1) w1v = ld16(w1 + (size_t)tid * FF + fb * 8);
  u32x4 woreg[4], w2reg[2];
  unsigned b1v;
  const int colw = wave * 128 + n * 8;
  const bool colok = colw < D;
  if (wave < 4) {
    const int colc = colok ? colw : colw - 64;
    const u16* wop = wo + (size_t)(h * 512 + s * 16 + 4 * g) * D + colc;
#pragma unroll
    for (int t = 0; t < 4; ++t) woreg[t] = ld16nt(wop + t * D);
    if (has_mlp) {
      const u16* w2p = w2 + (size_t)(fb * 8 + 2 * g) * D + colc;
#pragma unroll
      for (int t = 0; t < 2; ++t) w2reg[t] = ld16nt(w2p + t * D);
      b1v = *reinterpret_cast<const unsigned*>(b1 + fb * 8 + 2 * g);
    }
  }
  __builtin_amdgcn_sched_barrier(0);

  {
    gu64* qg = (gu64*)(ws + WS_QG) + (h * 512 + tid);
    gu64* fp = (gu64*)(ws + WS_F) + (tid < NCONS ? tid : 0);
    u64 gr;
    for (unsigned spins = 0;; ++spins) {
      gr = __hip_atomic_load(qg, RLX_AGENT);
      u64 fl = epoch + 1ull;
      if (wave < 2) fl = __hip_atomic_load(fp, RLX_AGENT);
      const bool ok = (((gr ^ tagw) >> 16) | (fl ^ (epoch + 1ull))) == 0ull;
      if (__all(ok) || spins > SPIN_CAP) break;
      __builtin_amdgcn_s_sleep(1);
    }
    sm.qs[tid] = (u16)gr;
  }
  if (do_w1) {
    float xnf[8], s2 = 0.f;
#pragma unroll
    for (int i = 0; i < 8; ++i) { xnf[i] = rbf((xf[i] - mean) * rstd); s2 += xnf[i]; }
    if (lane >= 56) s2 = 0.f;
    const float mean2 = wave_sum(s2) * (1.f / D);
    float q2 = 0.f;
#pragma unroll
    for (int i = 0; i < 8; ++i) { const float d = xnf[i] - mean2; q2 += d * d; }
    if (lane >= 56) q2 = 0.f;
    const float rstd2 = 1.f / sqrtf(wave_sum(q2) * (1.f / D) + EPS);
    const float xnt = rbf((bf2f(xrt) - mean) * rstd);
    const float yt = rbf((xnt - mean2) * rstd2);
    float a[8];
#pragma unroll
    for (int i = 0; i < 4; ++i) { a[2 * i] = yt * bf_lo(w1v[i]); a[2 * i + 1] = yt * bf_hi(w1v[i]); }
    float c4[4], c2[2];
#pragma unroll
    for (int i = 0; i < 4; ++i) c4[i] = fold32(a[i], a[i + 4]);
#pragma unroll
    for (int i = 0; i < 2; ++i) c2[i] = row_sum(fold16(c4[i], c4[i + 2]));
    if (n == 0) {
      const int f = 2 * (g & 1) + 4 * (g >> 1);
      sm.hpart[wave][f] = c2[0]; sm.hpart[wave][f + 1] = c2[1];
    }
  }
  __syncthreads();

  u32x4 pm;
  {
    const unsigned half = (lane & 1) ? 0xffff0000u : 0x0000ffffu;
#pragma unroll
    for (int i = 0; i < 4; ++i) pm[i] = (((lane & 7) >> 1) == i) ? half : 0u;
  }
  {
    f32x4 sc = {0.f, 0.f, 0.f, 0.f};
    const u16* qp = sm.qs + (n >> 3) * 256 + g * 8;
#pragma unroll
    for (int st = 0; st < 8; ++st) {
      const u32x4 qf = *reinterpret_cast<const u32x4*>(qp + st * 32);
      sc = mfma(kreg[st], qf, sc);
    }
    float sv[4];
#pragma unroll
    for (int t = 0; t < 4; ++t) sv[t] = sc[t] * SCALE_LOG2E;
    const float m = xrow_max(fmaxf(fmaxf(sv[0], sv[1]), fmaxf(sv[2], sv[3])));
    float p[4];
#pragma unroll
    for (int t = 0; t < 4; ++t) p[t] = __builtin_amdgcn_exp2f(sv[t] - m);
    const float l = xrow_sum((p[0] + p[1]) + (p[2] + p[3]));
    f32x4 oacc[2] = {{0.f, 0.f, 0.f, 0.f}, {0.f, 0.f, 0.f, 0.f}};
#pragma unroll
    for (int t = 0; t < 4; ++t) {
      const unsigned pd = cvtpk(p[t], p[t]);
      const u32x4 af = {pd & pm[0], pd & pm[1], pd & pm[2], pd & pm[3]};
      oacc[0] = mfma(af, vreg[t][0], oacc[0]);
      oacc[1] = mfma(af, vreg[t][1], oacc[1]);
    }
    float* ob = &sm.obuf[wave][(g >> 1) * 256 + 8 * n + 4 * (g & 1)];
    *reinterpret_cast<f32x4*>(ob) = oacc[0];
    *reinterpret_cast<f32x4*>(ob + 128) = oacc[1];
    if (lane == 0 || lane == 8) { sm.stats[wave][(lane >> 3) * 2] = m; sm.stats[wave][(lane >> 3) * 2 + 1] = l; }
  }
  __syncthreads();
  if (tid < 128) {
    const int hd = tid >> 6;
    float mw[8], M = -INFINITY;
#pragma unroll
    for (int w = 0; w < 8; ++w) { mw[w] = sm.stats[w][hd * 2]; M = fmaxf(M, mw[w]); }
    f32x4 o = {0.f, 0.f, 0.f, 0.f};
#pragma unroll
    for (int w = 0; w < 8; ++w) {
      const float wt = __builtin_amdgcn_exp2f(mw[w] - M);
      o += wt * *reinterpret_cast<const f32x4*>(&sm.obuf[w][tid * 4]);
    }
    __builtin_amdgcn_raw_buffer_store_b128(san4(o), rs,
        WS_S2D + (unsigned)((h * 32 + (tid >> 2)) * 32 + s) * 64u + (tid & 3) * 16u, 0, 16);
  } else if (tid == 128) {
    f32x4 st;
#pragma unroll
    for (int hd = 0; hd < 2; ++hd) {
      float M = -INFINITY, L = 0.f;
#pragma unroll
      for (int w = 0; w < 8; ++w) M = fmaxf(M, sm.stats[w][hd * 2]);
#pragma unroll
      for (int w = 0; w < 8; ++w) L += __builtin_amdgcn_exp2f(sm.stats[w][hd * 2] - M) * sm.stats[w][hd * 2 + 1];
      st[hd * 2] = M; st[hd * 2 + 1] = L;
    }
    __builtin_amdgcn_raw_buffer_store_b128(san4(st), rs, WS_S2S + (unsigned)(h * 32 + s) * 16u, 0, 16);
  }

  if (wave < 4) {
    f32x4 acc = {0.f, 0.f, 0.f, 0.f};
    if (has_mlp) {
#pragma unroll
      for (int t = 0; t < 2; ++t) {
        float hv = 0.f;
#pragma unroll
        for (int w = 0; w < 7; ++w) hv += sm.hpart[w][2 * g + t];
        hv += t ? bf_hi(b1v) : bf_lo(b1v);
        const float u = 0.7978845608028654f * (hv + 0.044715f * hv * hv * hv);
        const float ge = hv / (1.f + __expf(-2.f * u));
        const unsigned hd = cvtpk(ge, ge);
        const u32x4 af = {hd & pm[0], hd & pm[1], hd & pm[2], hd & pm[3]};
        acc = mfma(af, w2reg[t], acc);
      }
    }
    u32x4 d0, d1, s0, s1;
    {
      const unsigned doff = WS_S2D + (unsigned)(h * 32 + s) * 2048u + n * 64u + g * 16u;
      const unsigned soff = WS_S2S + (unsigned)(h * 32 + n) * 16u;
      for (unsigned spins = 0;; ++spins) {
        asm volatile("" ::: "memory");
        d0 = __builtin_amdgcn_raw_buffer_load_b128(rs, doff, 0, 16);
        d1 = __builtin_amdgcn_raw_buffer_load_b128(rs, doff + 1024u, 0, 16);
        s0 = __builtin_amdgcn_raw_buffer_load_b128(rs, soff, 0, 16);
        s1 = __builtin_amdgcn_raw_buffer_load_b128(rs, soff + 256u, 0, 16);
        const bool ok = max(max(umax4(d0), umax4(d1)), max(umax4(s0), umax4(s1))) != SENT;
        if (__all(ok) || spins > SPIN_CAP) break;
        __builtin_amdgcn_s_sleep(1);
      }
    }
    const bool hi = (s >> 4) != 0;
    const float m0 = asf(hi ? s0[2] : s0[0]), l0 = asf(hi ? s0[3] : s0[1]);
    const float m1 = asf(hi ? s1[2] : s1[0]), l1 = asf(hi ? s1[3] : s1[1]);
    const float M = row_max(fmaxf(m0, m1));
    const float e0 = __builtin_amdgcn_exp2f(m0 - M), e1 = __builtin_amdgcn_exp2f(m1 - M);
    const float inv = 1.f / row_sum(e0 * l0 + e1 * l1);
#pragma unroll
    for (int t = 0; t < 4; ++t) {
      const float o = row_sum(e0 * asf(d0[t]) + e1 * asf(d1[t])) * inv;
      const unsigned od = cvtpk(o, o);
      const u32x4 af = {od & pm[0], od & pm[1], od & pm[2], od & pm[3]};
      acc = mfma(af, woreg[t], acc);
    }
    if (g < 2 && colok)
      __builtin_amdgcn_raw_buffer_store_b128(san4(acc), rs,
          WS_S3 + (unsigned)(wave * 32 + 2 * n + g) * 4096u + (unsigned)b * 16u, 0, 16);
  } else if (wave == 4 && b < NCONS) {
    const int col = 4 * b + 2 * (g >> 1) + (g & 1);
    const float base = bf2f(x[col]) + bf2f(b2[col]);
    u32x4 v[4];
    const unsigned off = WS_S3 + (unsigned)b * 4096u + lane * 16u;
    for (unsigned spins = 0;; ++spins) {
      asm volatile("" ::: "memory");
#pragma unroll
      for (int i = 0; i < 4; ++i) v[i] = __builtin_amdgcn_raw_buffer_load_b128(rs, off + i * 1024u, 0, 16);
      const bool ok = max(max(umax4(v[0]), umax4(v[1])), max(umax4(v[2]), umax4(v[3]))) != SENT;
      if (__all(ok) || spins > SPIN_CAP) break;
      __builtin_amdgcn_s_sleep(1);
    }
    float a[4];
#pragma unroll
    for (int i = 0; i < 4; ++i) a[i] = (asf(v[0][i]) + asf(v[1][i])) + (asf(v[2][i]) + asf(v[3][i]));
    const float c0 = fold32(a[0], a[2]), c1 = fold32(a[1], a[3]);
    const float r = row_sum(fold16(c0, c1));
    if (n == 0) out[col] = (u16)(cvtpk(r + base, r + base) & 0xffffu);
    if (b == 0 && lane == 0) __hip_atomic_store((gu64*)(ws + WS_E), epoch + 1ull, RLX_AGENT);
  }
}

}

extern "C" void kernel_launch(void* const* d_in, const int* in_sizes, int n_in,
                              void* d_out, int out_size, void* d_ws, size_t ws_size,
                              hipStream_t stream) {
  (void)in_sizes; (void)n_in; (void)out_size; (void)ws_size;
  const u16* x = (const u16*)d_in[0];
  const u16* kc = (const u16*)d_in[1];
  const u16* vc = (const u16*)d_in[2];
  const u16* wq = (const u16*)d_in[3];
  const u16* wo = (const u16*)d_in[4];
  const u16* w1 = (const u16*)d_in[6];
  const u16* b1 = (const u16*)d_in[7];
  const u16* w2 = (const u16*)d_in[8];
  const u16* b2 = (const u16*)d_in[9];
  fused_kernel<<<dim3(NWG), dim3(NTHR), 0, stream>>>(x, kc, vc, wq, wo, w1, b1, w2, b2, (u16*)d_out,
                                                     (unsigned char*)d_ws);
}
```

```cpp
#include <hip/hip_runtime.h>
#include <hip/hip_bf16.h>
#include <stdint.h>

#define GAS __attribute__((address_space(1)))
typedef unsigned u32x4 __attribute__((ext_vector_type(4)));
typedef float f32x4 __attribute__((ext_vector_type(4)));
typedef __bf16 bf16x8 __attribute__((ext_vector_type(8)));
typedef __bf16 bf16x2 __attribute__((ext_vector_type(2)));
typedef unsigned short u16;
typedef unsigned long long u64;
typedef GAS u64 gu64;
#define RLX_AGENT __ATOMIC_RELAXED, __HIP_MEMORY_SCOPE_AGENT
#define RLX_WG __ATOMIC_RELAXED, __HIP_MEMORY_SCOPE_WORKGROUP

#define TR(k) do {} while (0)
#define TR_INIT() do {} while (0)
#define TR_DUMP() do {} while (0)

namespace {

constexpr int D = 448, SK = 32768, DH = 256, WIN = 4096, FF = 1792;
constexpr int NQ = 4096;
constexpr int NWG = 256, NTHR = 1024;
constexpr int CONS0 = 224, NCONS = 28;
constexpr float EPS = 1e-5f;
constexpr float SCALE_LOG2E = 0.0625f * 1.4426950408889634f;
constexpr unsigned SENT = 0xFFFFFFFFu;
constexpr unsigned SPIN_CAP = 100000u;
constexpr unsigned LSPIN_CAP = 4000000u;

constexpr unsigned WS_E = 0;
constexpr unsigned WS_F = 4096;
constexpr unsigned WS_QG = 8192;
constexpr unsigned WS_HG = 147456;
constexpr unsigned WS_S2S = 262144;
constexpr unsigned WS_S2D = 524288;
constexpr unsigned WS_S3 = 1048576;
constexpr unsigned WS_BYTES = WS_S3 + NWG * 2048;

__device__ __forceinline__ float bf_lo(unsigned v) { return __uint_as_float(v << 16); }
__device__ __forceinline__ float bf_hi(unsigned v) { return __uint_as_float(v & 0xffff0000u); }
__device__ __forceinline__ float bf2f(u16 v) { return __uint_as_float((unsigned)v << 16); }
__device__ __forceinline__ unsigned cvtpk(float lo, float hi) {
  const bf16x2 v = {(__bf16)lo, (__bf16)hi};
  return __builtin_bit_cast(unsigned, v);
}
__device__ __forceinline__ float rbf(float x) { return bf_lo(cvtpk(x, x)); }

template <int CTRL> __device__ __forceinline__ float dpp(float x) {
  return __int_as_float(__builtin_amdgcn_mov_dpp(__float_as_int(x), CTRL, 0xf, 0xf, true));
}
constexpr int ROR1 = 0x121, ROR2 = 0x122, ROR4 = 0x124, ROR8 = 0x128, QXOR1 = 0xB1, QXOR2 = 0x4E, HMIRROR = 0x141;
__device__ __forceinline__ float row_sum(float x) {
  x += dpp<ROR8>(x); x += dpp<ROR4>(x); x += dpp<ROR2>(x); x += dpp<ROR1>(x); return x;
}
__device__ __forceinline__ float fold32(float a, float b) {
  auto r = __builtin_amdgcn_permlane32_swap(__float_as_uint(a), __float_as_uint(b), false, false);
  return __uint_as_float(r[0]) + __uint_as_float(r[1]);
}
__device__ __forceinline__ float fold16(float a, float b) {
  auto r = __builtin_amdgcn_permlane16_swap(__float_as_uint(a), __float_as_uint(b), false, false);
  return __uint_as_float(r[0]) + __uint_as_float(r[1]);
}
__device__ __forceinline__ float xrow_sum(float x) { x = fold16(x, x); return fold32(x, x); }
__device__ __forceinline__ float xrow_max(float x) {
  auto s = __builtin_amdgcn_permlane16_swap(__float_as_uint(x), __float_as_uint(x), false, false);
  x = fmaxf(__uint_as_float(s[0]), __uint_as_float(s[1]));
  auto t = __builtin_amdgcn_permlane32_swap(__float_as_uint(x), __float_as_uint(x), false, false);
  return fmaxf(__uint_as_float(t[0]), __uint_as_float(t[1]));
}
__device__ __forceinline__ float wave_sum(float x) { return xrow_sum(row_sum(x)); }

__device__ __forceinline__ u32x4 ld16(const u16* p) { return *reinterpret_cast<const u32x4*>(p); }
__device__ __forceinline__ __amdgpu_buffer_rsrc_t mkrs(const void* p, unsigned bytes) {
  return __builtin_amdgcn_make_buffer_rsrc(const_cast<void*>(p), 0, bytes, 0x00020000);
}
__device__ __forceinline__ u32x4 ldnt(__amdgpu_buffer_rsrc_t r, unsigned elem) { return __builtin_amdgcn_raw_buffer_load_b128(r, elem * 2u, 0, 2); }
__device__ __forceinline__ f32x4 mfma(u32x4 a, u32x4 b, f32x4 c) {
  return __builtin_amdgcn_mfma_f32_16x16x32_bf16(__builtin_bit_cast(bf16x8, a), __builtin_bit_cast(bf16x8, b), c, 0, 0, 0);
}
__device__ __forceinline__ u32x4 place(unsigned v, u32x4 pm) { return u32x4{v & pm[0], v & pm[1], v & pm[2], v & pm[3]}; }
__device__ __forceinline__ unsigned san(float v) {
  const unsigned b = __float_as_uint(v);
  return b == SENT ? 0x7fc00000u : b;
}
__device__ __forceinline__ u32x4 san4(f32x4 v) { return u32x4{san(v[0]), san(v[1]), san(v[2]), san(v[3])}; }
__device__ __forceinline__ unsigned umax4(u32x4 v) { return max(max(v[0], v[1]), max(v[2], v[3])); }
__device__ __forceinline__ float asf(unsigned v) { return __uint_as_float(v); }

enum { FL_QP = 0, FL_QPUB, FL_Q, FL_HP, FL_HPUB, FL_CLR, FL_ST, FL_ISS, FL_PART, FL_H, FL_F, FL_O, FL_S3PUB, FL_N };

struct Smem {
  float obuf[8][512];
  float qpart[14][64];
  unsigned qs[256];
  float hpart[14][64];
  float ew[32];
  float wmax[2][8];
  float wsum[2][8];
  unsigned hd[8];
  unsigned od[16];
  float fin[4][16];
  u64 flags[16];
  u64 eall[8];
  unsigned fincnt;
};

__device__ __forceinline__ void lds_signal(u64* f, int idx) {
  asm volatile("s_waitcnt lgkmcnt(0)" ::: "memory");
  __hip_atomic_store(reinterpret_cast<unsigned char*>(f) + idx, (unsigned char)1, RLX_WG);
}
constexpr u64 ALL8 = 0x0101010101010101ull, ALL7 = 0x0001010101010101ull, ALL4 = 0x01010101ull, ALL1 = 1ull;
template <int SLEEP>
__device__ __forceinline__ void lds_wait(u64* f, u64 mask) {
#pragma nounroll
  for (unsigned spins = 0; spins < LSPIN_CAP; ++spins) {
    if (!__any((__hip_atomic_load(f, RLX_WG) & mask) != mask)) break;
    if (SLEEP) __builtin_amdgcn_s_sleep(SLEEP);
  }
  asm volatile("" ::: "memory");
}
template <int N> __device__ __forceinline__ void waitvm() { asm volatile("s_waitcnt vmcnt(%0)" :: "i"(N) : "memory"); }
__device__ __forceinline__ void wg_barrier() {
  __builtin_amdgcn_sched_barrier(0);
  asm volatile("s_waitcnt lgkmcnt(0)" ::: "memory");
  __builtin_amdgcn_s_barrier();
  asm volatile("" ::: "memory");
  __builtin_amdgcn_sched_barrier(0);
}
#define SWIN 3
#define SPRE 1
#define STHR 12
#define SDELAY 24
#define QDELAY 12

__device__ __forceinline__ void ln_stats(const float (&v)[8], int lane, float& mean, float& rstd) {
  float sum = ((v[0] + v[1]) + (v[2] + v[3])) + ((v[4] + v[5]) + (v[6] + v[7]));
  if (lane >= 56) sum = 0.f;
  mean = wave_sum(sum) * (1.f / D);
  float sq = 0.f;
#pragma unroll
  for (int i = 0; i < 8; ++i) { const float d = v[i] - mean; sq = fmaf(d, d, sq); }
  if (lane >= 56) sq = 0.f;
  rstd = __builtin_amdgcn_rsqf(wave_sum(sq) * (1.f / D) + EPS);
}

__global__ void __launch_bounds__(NTHR, 4)
fused_kernel(const u16* __restrict__ x, const u16* __restrict__ kc, const u16* __restrict__ vc,
             const u16* __restrict__ wq, const u16* __restrict__ wo, const u16* __restrict__ w1,
             const u16* __restrict__ b1, const u16* __restrict__ w2, const u16* __restrict__ b2,
             u16* __restrict__ out, unsigned char* ws) {
  __shared__ __attribute__((aligned(16))) Smem sm;
  const int tid = threadIdx.x, lane = tid & 63;
  const int wave = __builtin_amdgcn_readfirstlane(tid >> 6);
  const int b = blockIdx.x, h = b & 7, s = b >> 3;
  const int g = lane >> 4, n = lane & 15;
  if (tid >= 512 && tid < 528) sm.flags[tid - 512] = 0ull;
  if (tid == 528) sm.fincnt = 0u;
  TR_INIT();
  TR(0);
  asm volatile("" :: "s"(x), "s"(kc), "s"(vc), "s"(wq), "s"(wo), "s"(w1), "s"(b1), "s"(w2), "s"(b2), "s"(out), "s"(ws));

  u32x4 pm;
  {
    const unsigned half = (lane & 1) ? 0xffff0000u : 0x0000ffffu;
#pragma unroll
    for (int i = 0; i < 4; ++i) pm[i] = (((lane & 7) >> 1) == i) ? half : 0u;
  }

  if (wave >= 8) {
    const int sw = wave - 8;
    const size_t kvbase = ((size_t)h * SK + (SK - WIN) + s * 128 + sw * 16) * DH;
    const __amdgpu_buffer_rsrc_t rk = mkrs(kc + kvbase, 16 * DH * 2), rv = mkrs(vc + kvbase, 16 * DH * 2);
    const unsigned ko = (unsigned)((n & 12) * DH + ((n & 3) + 4 * g) * 8), vo = (unsigned)(4 * g * DH + n * 8);
    u32x4 kv[16];
#pragma unroll
    for (int i = 0; i < 16; ++i) {
      if (i == SPRE) { TR(1); wg_barrier(); TR(2); }
      if (i >= SWIN && i < STHR) waitvm<SWIN - 1>();
      kv[i] = i < 8 ? ldnt(rk, ko + (i >> 1) * DH + (i & 1) * 128) : ldnt(rv, vo + ((i - 8) >> 1) * DH + (i & 1) * 128);
    }
    __builtin_amdgcn_sched_barrier(0);
    lds_signal(&sm.flags[FL_ISS], sw);
    TR(3);
    lds_wait<0>(&sm.flags[FL_Q], ALL4);
    TR(4);
    const u32x4 qf0 = *reinterpret_cast<const u32x4*>(&sm.qs[(n >> 3) * 128 + ((n & 3) + 4 * g) * 4]);
    const u32x4 qf1 = *reinterpret_cast<const u32x4*>(&sm.qs[(n >> 3) * 128 + ((n & 3) + 4 * g) * 4 + 64]);
    float sv[4];
#pragma unroll
    for (int t = 0; t < 4; ++t) {
      f32x4 d = mfma(kv[2 * t], qf0, f32x4{0.f, 0.f, 0.f, 0.f});
      d = mfma(kv[2 * t + 1], qf1, d);
      float v = (n & 2) ? ((n & 1) ? d[3] : d[2]) : ((n & 1) ? d[1] : d[0]);
      v += dpp<QXOR1>(v); v += dpp<QXOR2>(v);
      sv[t] = v * SCALE_LOG2E;
    }
    const float m = xrow_max(fmaxf(fmaxf(sv[0], sv[1]), fmaxf(sv[2], sv[3])));
    float p[4];
#pragma unroll
    for (int t = 0; t < 4; ++t) p[t] = __builtin_amdgcn_exp2f(sv[t] - m);
    const float l = xrow_sum((p[0] + p[1]) + (p[2] + p[3]));
    sm.wmax[n >> 3][sw] = m;
    sm.wsum[n >> 3][sw] = l;
    lds_signal(&sm.flags[FL_ST], sw);
    TR(5);
    u32x4 af[4];
#pragma unroll
    for (int t = 0; t < 4; ++t) af[t] = place(cvtpk(p[t], p[t]), pm);
    __builtin_amdgcn_sched_barrier(0);
    f32x4 o0 = {0.f, 0.f, 0.f, 0.f}, o1 = {0.f, 0.f, 0.f, 0.f};
#pragma unroll
    for (int t = 0; t < 4; ++t) { o0 = mfma(af[t], kv[8 + 2 * t], o0); o1 = mfma(af[t], kv[9 + 2 * t], o1); }
    float* ob = &sm.obuf[sw][(g >> 1) * 256 + 8 * n + 4 * (g & 1)];
    *reinterpret_cast<f32x4*>(ob) = o0;
    *reinterpret_cast<f32x4*>(ob + 128) = o1;
    lds_signal(&sm.flags[FL_PART], sw);
    TR(7);
    TR_DUMP();
    return;
  }

  const int aw = wave;
  const __amdgpu_buffer_rsrc_t rs = __builtin_amdgcn_make_buffer_rsrc(ws, 0, WS_BYTES, 0x00020000);
  const unsigned rec = (unsigned)(h * 32 + s);
  const bool has_mlp = s < 28;
  const bool is_cons = b >= CONS0 && b < CONS0 + NCONS;
  {
    const u32x4 xv = ld16(x + 8 * (lane < 56 ? lane : 0));
    if (aw < 7) {
      const int row = (s >> 3) * 112 + aw * 16 + (lane >> 3);
      const int hrow = h * 56 + aw * 8 + (lane >> 3);
      const __amdgpu_buffer_rsrc_t rq = mkrs(wq, D * NQ * 2);
      const unsigned wqo = (unsigned)(row * NQ + h * 512 + (s & 7) * 64 + (lane & 7) * 8);
      const u16 xr0 = x[row], xr1 = x[row + 8], xrh = x[hrow];
      __builtin_amdgcn_sched_barrier(0);
      const u32x4 wq0 = ldnt(rq, wqo), wq1 = ldnt(rq, wqo + 8 * NQ);
      TR(1);
      wg_barrier();
      TR(2);
      float xf[8], mean, rstd;
#pragma unroll
      for (int i = 0; i < 4; ++i) { xf[2 * i] = bf_lo(xv[i]); xf[2 * i + 1] = bf_hi(xv[i]); }
      ln_stats(xf, lane, mean, rstd);
      const float n0 = (bf2f(xr0) - mean) * rstd, n1 = (bf2f(xr1) - mean) * rstd;
      f32x4 a = mfma(place(cvtpk(n0, n0), pm), wq0, f32x4{0.f, 0.f, 0.f, 0.f});
      a = mfma(place(cvtpk(n1, n1), pm), wq1, a);
      const bool use = (n >> 3) == (g >> 1);
      const int pidx = (2 * aw + (g >> 1)) * 64 + 8 * (n & 7) + 4 * (g & 1);
      if (use) *reinterpret_cast<f32x4*>(&sm.qpart[0][pidx]) = a;
      lds_signal(&sm.flags[FL_QP], aw);
      TR(3);
      u32x4 w1v;
      if (has_mlp) w1v = ldnt(mkrs(w1, D * FF * 2), (unsigned)(hrow * FF + s * 64 + (lane & 7) * 8));
      if (aw == 0) {
        lds_wait<0>(&sm.flags[FL_QP], ALL8);
        float t[14];
#pragma unroll
        for (int i = 0; i < 14; ++i) t[i] = sm.qpart[i][lane];
        const float qv = (((t[0] + t[1]) + (t[2] + t[3])) + ((t[4] + t[5]) + (t[6] + t[7]))) + (((t[8] + t[9]) + (t[10] + t[11])) + (t[12] + t[13]));
        const u64 gran = (sm.eall[h] << 24) | (u64)(__float_as_uint(qv) >> 8);
        __hip_atomic_store((gu64*)(ws + WS_QG) + ((h * 4 + (s >> 3)) * 512 + (s & 7) * 64 + lane), gran, RLX_AGENT);
        lds_signal(&sm.flags[FL_QPUB], 0);
        TR(13);
      }
      if (has_mlp) {
        float mean2, rstd2;
#pragma unroll
        for (int i = 0; i < 8; ++i) xf[i] = rbf((xf[i] - mean) * rstd);
        ln_stats(xf, lane, mean2, rstd2);
        const float yv = (rbf((bf2f(xrh) - mean) * rstd) - mean2) * rstd2;
        const f32x4 hp = mfma(place(cvtpk(yv, yv), pm), w1v, f32x4{0.f, 0.f, 0.f, 0.f});
        if (use) *reinterpret_cast<f32x4*>(&sm.hpart[0][pidx]) = hp;
        lds_signal(&sm.flags[FL_HP], aw);
        TR(5);
      }
    } else {
      const u64 e0 = __hip_atomic_load((gu64*)(ws + WS_E + (lane & 7) * 256), RLX_AGENT);
      const u32x4 sent4 = {SENT, SENT, SENT, SENT};
      const unsigned o2 = WS_S2D + rec * 2048u + lane * 16u, o3 = WS_S3 + (unsigned)b * 2048u + lane * 16u;
      __builtin_amdgcn_raw_buffer_store_b128(sent4, rs, o2, 0, 16);
      __builtin_amdgcn_raw_buffer_store_b128(sent4, rs, o2 + 1024u, 0, 16);
      __builtin_amdgcn_raw_buffer_store_b128(sent4, rs, o3, 0, 16);
      __builtin_amdgcn_raw_buffer_store_b128(sent4, rs, o3 + 1024u, 0, 16);
      TR(1);
      wg_barrier();
      TR(2);
      const u64 e1 = e0 + 1ull;
      sm.eall[lane & 7] = e1;
      lds_signal(&sm.flags[FL_QP], 7);
      TR(3);
      waitvm<0>();
      lds_signal(&sm.flags[FL_CLR], 0);
      if (lane == h) __hip_atomic_store((gu64*)(ws + WS_F) + b, e1, RLX_AGENT);
      TR(13);
      if (has_mlp) {
        lds_wait<1>(&sm.flags[FL_HP], ALL7);
        float t[14];
#pragma unroll
        for (int i = 0; i < 14; ++i) t[i] = sm.hpart[i][lane];
        const float hv = (((t[0] + t[1]) + (t[2] + t[3])) + ((t[4] + t[5]) + (t[6] + t[7]))) + (((t[8] + t[9]) + (t[10] + t[11])) + (t[12] + t[13]));
        const u64 gran = (sm.eall[h] << 24) | (u64)(__float_as_uint(hv) >> 8);
        __hip_atomic_store((gu64*)(ws + WS_HG) + (b * 64 + lane), gran, RLX_AGENT);
        lds_signal(&sm.flags[FL_HPUB], 0);
        TR(5);
      }
    }
  }
  if (aw >= 1 && aw <= 4) {
    lds_wait<1>(&sm.flags[FL_QPUB], ALL1);
    __builtin_amdgcn_s_sleep(QDELAY);
    const u64 epoch1 = sm.eall[h];
    const unsigned tag_lo = (unsigned)epoch1 & 0xffu, tag_hi = (unsigned)(epoch1 >> 8);
    const unsigned qoff = WS_QG + (unsigned)(h * 2048 + (aw - 1) * 128 + 2 * lane) * 8u;
    u32x4 gq[4];
#pragma nounroll
    for (unsigned spins = 0; spins < SPIN_CAP; ++spins) {
      asm volatile("" ::: "memory");
      unsigned bad = 0u;
#pragma unroll
      for (int i = 0; i < 4; ++i) gq[i] = __builtin_amdgcn_raw_buffer_load_b128(rs, qoff + i * 4096u, 0, 16);
#pragma unroll
      for (int i = 0; i < 4; ++i) bad |= ((gq[i][0] >> 24) ^ tag_lo) | (gq[i][1] ^ tag_hi) | ((gq[i][2] >> 24) ^ tag_lo) | (gq[i][3] ^ tag_hi);
      if (!__any(bad != 0u)) break;
      __builtin_amdgcn_s_sleep(12);
    }
    const float q0 = (asf(gq[0][0] << 8) + asf(gq[1][0] << 8)) + (asf(gq[2][0] << 8) + asf(gq[3][0] << 8));
    const float q1 = (asf(gq[0][2] << 8) + asf(gq[1][2] << 8)) + (asf(gq[2][2] << 8) + asf(gq[3][2] << 8));
    sm.qs[(aw - 1) * 64 + lane] = cvtpk(q0, q1);
    lds_signal(&sm.flags[FL_Q], aw - 1);
    TR(4);
  }

  if (aw < 4) {
    u32x4 woreg[4], w2reg[2];
    const int colw = aw * 128 + n * 8;
    const bool colok = colw < D;
    lds_wait<2>(&sm.flags[FL_ISS], ALL8);
    TR(9);
    {
      const int colc = colok ? colw : colw - 64;
      const __amdgpu_buffer_rsrc_t ro = mkrs(wo + (size_t)(h * 512 + s * 16) * D, 16 * D * 2);
#pragma unroll
      for (int t = 0; t < 4; ++t) woreg[t] = ldnt(ro, (unsigned)((4 * g + t) * D + colc));
      if (has_mlp) {
        const __amdgpu_buffer_rsrc_t r2 = mkrs(w2 + (size_t)(b * 8) * D, 8 * D * 2);
#pragma unroll
        for (int t = 0; t < 2; ++t) w2reg[t] = ldnt(r2, (unsigned)((2 * g + t) * D + colc));
      }
    }
    __builtin_amdgcn_sched_barrier(0);
    f32x4 acc = {0.f, 0.f, 0.f, 0.f};
    if (has_mlp) {
      lds_wait<1>(&sm.flags[FL_H], ALL1);
#pragma unroll
      for (int t = 0; t < 2; ++t) acc = mfma(place(sm.hd[2 * g + t], pm), w2reg[t], acc);
    }
    asm volatile("" :: "v"(woreg[0]), "v"(woreg[1]), "v"(woreg[2]), "v"(woreg[3]));
    TR(10);
    lds_wait<0>(&sm.flags[FL_O], ALL1);
    TR(11);
    const u32x4 od = *reinterpret_cast<const u32x4*>(&sm.od[4 * g]);
    f32x4 acc2 = mfma(place(od[1], pm), woreg[1], f32x4{0.f, 0.f, 0.f, 0.f});
    acc = mfma(place(od[0], pm), woreg[0], acc);
    acc2 = mfma(place(od[3], pm), woreg[3], acc2);
    acc = mfma(place(od[2], pm), woreg[2], acc);
    acc += acc2;
    if (g < 2 && colok)
      __builtin_amdgcn_raw_buffer_store_b128(san4(acc), rs, WS_S3 + (unsigned)b * 2048u + (unsigned)(colw + 4 * g) * 4u, 0, 16);
    if (is_cons) lds_signal(&sm.flags[FL_S3PUB], aw);
    TR(12);
  } else {
    if (aw == 4) {
      if (has_mlp) {
        const unsigned b1v = *reinterpret_cast<const unsigned*>(b1 + b * 8 + 2 * (lane & 3));
        const int pr = (lane >> 2) & 7;
        lds_wait<2>(&sm.flags[FL_HPUB], ALL1);
        lds_wait<2>(&sm.flags[FL_ISS], ALL1);
        const u64 ep = sm.eall[pr];
        const unsigned tag_lo = (unsigned)ep & 0xffu, tag_hi = (unsigned)(ep >> 8);
        const unsigned hoff = WS_HG + (unsigned)((s * 8 + pr) * 64 + h * 8 + 2 * (lane & 3)) * 8u;
        TR(9);
        u32x4 gh;
#pragma nounroll
        for (unsigned spins = 0; spins < SPIN_CAP; ++spins) {
          asm volatile("" ::: "memory");
          gh = __builtin_amdgcn_raw_buffer_load_b128(rs, hoff, 0, 16);
          const unsigned bad = ((gh[0] >> 24) ^ tag_lo) | (gh[1] ^ tag_hi) | ((gh[2] >> 24) ^ tag_lo) | (gh[3] ^ tag_hi);
          if (!__any(bad != 0u)) break;
          __builtin_amdgcn_s_sleep(12);
        }
        float hv = fold16(asf(gh[0] << 8), asf(gh[2] << 8));
        hv += dpp<ROR4>(hv); hv += dpp<ROR8>(hv);
        hv += (g & 1) ? bf_hi(b1v) : bf_lo(b1v);
        const float u = 0.7978845608028654f * (hv + 0.044715f * hv * hv * hv);
        const float ge = hv / (1.f + __expf(-2.f * u));
        sm.hd[2 * (n & 3) + (g & 1)] = cvtpk(ge, ge);
        lds_signal(&sm.flags[FL_H], 0);
        TR(11);
      } else if (is_cons) {
        const u64 ea = sm.eall[(2 * lane) & 7], eb = sm.eall[(2 * lane + 1) & 7];
        lds_wait<2>(&sm.flags[FL_ISS], ALL1);
#pragma nounroll
        for (unsigned spins = 0; spins < SPIN_CAP; ++spins) {
          asm volatile("" ::: "memory");
          const u32x4 f0 = __builtin_amdgcn_raw_buffer_load_b128(rs, WS_F + lane * 16u, 0, 16);
          const u32x4 f1 = __builtin_amdgcn_raw_buffer_load_b128(rs, WS_F + 1024u + lane * 16u, 0, 16);
          const unsigned bad = (f0[0] ^ (unsigned)ea) | (f0[1] ^ (unsigned)(ea >> 32)) | (f0[2] ^ (unsigned)eb) | (f0[3] ^ (unsigned)(eb >> 32)) |
                               (f1[0] ^ (unsigned)ea) | (f1[1] ^ (unsigned)(ea >> 32)) | (f1[2] ^ (unsigned)eb) | (f1[3] ^ (unsigned)(eb >> 32));
          if (!__any(bad != 0u)) break;
          __builtin_amdgcn_s_sleep(12);
        }
        lds_signal(&sm.flags[FL_F], 0);
        TR(9);
      }
    } else if (aw < 7) {
      const int idx = (aw - 5) * 64 + lane;
      lds_wait<2>(&sm.flags[FL_CLR], ALL1);
      lds_wait<1>(&sm.flags[FL_ST], ALL8);
      if (aw == 5) {
        const float mw = sm.wmax[n >> 3][n & 7], lw = sm.wsum[n >> 3][n & 7];
        float M = mw;
        M = fmaxf(M, dpp<QXOR1>(M)); M = fmaxf(M, dpp<QXOR2>(M)); M = fmaxf(M, dpp<HMIRROR>(M));
        float L = __builtin_amdgcn_exp2f(mw - M) * lw;
        L += dpp<QXOR1>(L); L += dpp<QXOR2>(L); L += dpp<HMIRROR>(L);
        const unsigned bits = __float_as_uint((n & 2) ? L : M);
        const u64 gran = (sm.eall[h] << 16) | (u64)((n & 1) ? bits >> 16 : bits & 0xffffu);
        if (lane < 16 && (n & 4) == 0)
          __hip_atomic_store((gu64*)(ws + WS_S2S + rec * 128u) + ((n >> 3) * 4 + (n & 3)), gran, RLX_AGENT);
        TR(9);
      }
      const f32x4 ma = *reinterpret_cast<const f32x4*>(&sm.wmax[aw - 5][0]);
      const f32x4 mb = *reinterpret_cast<const f32x4*>(&sm.wmax[aw - 5][4]);
      const float M = fmaxf(fmaxf(fmaxf(ma[0], ma[1]), fmaxf(ma[2], ma[3])), fmaxf(fmaxf(mb[0], mb[1]), fmaxf(mb[2], mb[3])));
      float wt[8];
#pragma unroll
      for (int w = 0; w < 4; ++w) { wt[w] = __builtin_amdgcn_exp2f(ma[w] - M); wt[4 + w] = __builtin_amdgcn_exp2f(mb[w] - M); }
      lds_wait<0>(&sm.flags[FL_PART], ALL8);
      TR(10);
      f32x4 o0 = {0.f, 0.f, 0.f, 0.f}, o1 = {0.f, 0.f, 0.f, 0.f};
#pragma unroll
      for (int w = 0; w < 8; w += 2) {
        o0 += wt[w] * *reinterpret_cast<const f32x4*>(&sm.obuf[w][idx * 4]);
        o1 += wt[w + 1] * *reinterpret_cast<const f32x4*>(&sm.obuf[w + 1][idx * 4]);
      }
      __builtin_amdgcn_raw_buffer_store_b128(san4(o0 + o1), rs, WS_S2D + rec * 2048u + idx * 16u, 0, 16);
      TR(11);
    } else {
      lds_wait<2>(&sm.flags[FL_ST], ALL8);
      __builtin_amdgcn_s_sleep(SDELAY);
      TR(9);
      float e0, e1;
      {
        const u64 ep = sm.eall[h];
        const unsigned tag_lo = (unsigned)ep & 0xffffu, tag_hi = (unsigned)(ep >> 16);
        const unsigned soff = WS_S2S + (unsigned)(h * 32 + (lane >> 1)) * 128u + (unsigned)(s >> 4) * 32u + (lane & 1) * 16u;
        u32x4 sg;
#pragma nounroll
        for (unsigned spins = 0; spins < SPIN_CAP; ++spins) {
          asm volatile("" ::: "memory");
          sg = __builtin_amdgcn_raw_buffer_load_b128(rs, soff, 0, 16);
          const unsigned bad = ((sg[0] >> 16) ^ tag_lo) | (sg[1] ^ tag_hi) | ((sg[2] >> 16) ^ tag_lo) | (sg[3] ^ tag_hi);
          if (!__any(bad != 0u)) break;
          __builtin_amdgcn_s_sleep(8);
        }
        const float v = asf((sg[0] & 0xffffu) | (sg[2] << 16)), vo = dpp<QXOR1>(v);
        const float mp = (lane & 1) ? vo : v, lp = (lane & 1) ? v : vo;
        float M = mp;
        M = fmaxf(M, dpp<ROR2>(M)); M = fmaxf(M, dpp<ROR4>(M)); M = fmaxf(M, dpp<ROR8>(M)); M = xrow_max(M);
        const float e = __builtin_amdgcn_exp2f(mp - M);
        float L = e * lp;
        L += dpp<ROR2>(L); L += dpp<ROR4>(L); L += dpp<ROR8>(L); L = xrow_sum(L);
        sm.ew[lane >> 1] = e / L;
        asm volatile("" ::: "memory");
        e0 = sm.ew[lane >> 2]; e1 = sm.ew[16 + (lane >> 2)];
      }
      TR(10);
      lds_wait<0>(&sm.flags[FL_PART], ALL8);
      __builtin_amdgcn_s_sleep(10);
      u32x4 d0 = {SENT, SENT, SENT, SENT}, d1 = {SENT, SENT, SENT, SENT};
      const unsigned doff = WS_S2D + (unsigned)(h * 32 + (lane >> 2)) * 2048u + s * 64u + (lane & 3) * 16u;
#pragma nounroll
      for (unsigned spins = 0; spins < SPIN_CAP; ++spins) {
        asm volatile("" ::: "memory");
        if (umax4(d0) == SENT) d0 = __builtin_amdgcn_raw_buffer_load_b128(rs, doff, 0, 16);
        if (umax4(d1) == SENT) d1 = __builtin_amdgcn_raw_buffer_load_b128(rs, doff + 32768u, 0, 16);
        if (!__any(max(umax4(d0), umax4(d1)) == SENT)) break;
      }
      TR(11);
      float v[4];
#pragma unroll
      for (int t = 0; t < 4; ++t) v[t] = e0 * asf(d0[t]) + e1 * asf(d1[t]);
      float o = fold16(fold32(v[0], v[2]), fold32(v[1], v[3]));
      o += dpp<ROR4>(o); o += dpp<ROR8>(o);
      sm.od[4 * (n & 3) + (g & 1) + 2 * (g >> 1)] = cvtpk(o, o);
      lds_signal(&sm.flags[FL_O], 0);
      TR(12);
    }
    if (is_cons) {
      const int pw = aw - 4;
      const u16 fin_x = x[(b - CONS0) * 16 + n], fin_b = b2[(b - CONS0) * 16 + n];
      lds_wait<1>(&sm.flags[FL_F], ALL1);
      lds_wait<0>(&sm.flags[FL_S3PUB], ALL4);
      __builtin_amdgcn_s_sleep(10);
      u32x4 v[4];
#pragma unroll
      for (int i = 0; i < 4; ++i) v[i] = u32x4{SENT, SENT, SENT, SENT};
      const unsigned off = WS_S3 + (unsigned)(pw * 64 + (lane >> 2)) * 2048u + (unsigned)(b - CONS0) * 64u + (lane & 3) * 16u;
#pragma nounroll
      for (unsigned spins = 0; spins < SPIN_CAP; ++spins) {
        asm volatile("" ::: "memory");
#pragma unroll
        for (int i = 0; i < 4; ++i)
          if (umax4(v[i]) == SENT) v[i] = __builtin_amdgcn_raw_buffer_load_b128(rs, off + i * 32768u, 0, 16);
        if (!__any(max(max(umax4(v[0]), umax4(v[1])), max(umax4(v[2]), umax4(v[3]))) == SENT)) break;
      }
      TR(14);
      float a[4];
#pragma unroll
      for (int i = 0; i < 4; ++i) a[i] = (asf(v[0][i]) + asf(v[1][i])) + (asf(v[2][i]) + asf(v[3][i]));
      float r = fold16(fold32(a[0], a[2]), fold32(a[1], a[3]));
      r += dpp<ROR4>(r); r += dpp<ROR8>(r);
      sm.fin[pw][4 * (n & 3) + (g & 1) + 2 * (g >> 1)] = r;
      asm volatile("s_waitcnt lgkmcnt(0)" ::: "memory");
      unsigned old = 0;
      if (lane == 0) old = __hip_atomic_fetch_add(&sm.fincnt, 1u, RLX_WG);
      old = __builtin_amdgcn_readfirstlane(old);
      asm volatile("" ::: "memory");
      if (old == 3u) {
        if (lane < 16) {
          const float t = ((sm.fin[0][n] + sm.fin[1][n]) + (sm.fin[2][n] + sm.fin[3][n])) + (bf2f(fin_x) + bf2f(fin_b));
          out[(b - CONS0) * 16 + n] = (u16)cvtpk(t, 0.f);
        }
        if (b == CONS0 && lane < 8) __hip_atomic_store((gu64*)(ws + WS_E + lane * 256), sm.eall[lane], RLX_AGENT);
        TR(15);
      }
    }
  }
  TR_DUMP();
}

}

extern "C" void kernel_launch(void* const* d_in, const int* in_sizes, int n_in,
                              void* d_out, int out_size, void* d_ws, size_t ws_size,
                              hipStream_t stream) {
  (void)in_sizes; (void)n_in; (void)out_size; (void)ws_size;
  const u16* x = (const u16*)d_in[0];
  const u16* kc = (const u16*)d_in[1];
  const u16* vc = (const u16*)d_in[2];
  const u16* wq = (const u16*)d_in[3];
  const u16* wo = (const u16*)d_in[4];
  const u16* w1 = (const u16*)d_in[6];
  const u16* b1 = (const u16*)d_in[7];
  const u16* w2 = (const u16*)d_in[8];
  const u16* b2 = (const u16*)d_in[9];
  fused_kernel<<<dim3(NWG), dim3(NTHR), 0, stream>>>(x, kc, vc, wq, wo, w1, b1, w2, b2, (u16*)d_out,
                                                     (unsigned char*)d_ws);
}
```

```cpp
#include <hip/hip_runtime.h>
#include <hip/hip_bf16.h>
#include <stdint.h>

#define GAS __attribute__((address_space(1)))
typedef unsigned u32x4 __attribute__((ext_vector_type(4)));
typedef unsigned u32x2 __attribute__((ext_vector_type(2)));
typedef float f32x4 __attribute__((ext_vector_type(4)));
typedef float f32x2 __attribute__((ext_vector_type(2)));
typedef __bf16 bf16x8 __attribute__((ext_vector_type(8)));
typedef __bf16 bf16x2 __attribute__((ext_vector_type(2)));
typedef unsigned short u16;
typedef unsigned long long u64;
typedef GAS u64 gu64;
#define RLX_AGENT __ATOMIC_RELAXED, __HIP_MEMORY_SCOPE_AGENT
#define RLX_WG __ATOMIC_RELAXED, __HIP_MEMORY_SCOPE_WORKGROUP

#define TR(k) do {} while (0)
#define TR_INIT() do {} while (0)
#define TR_DUMP() do {} while (0)

namespace {

constexpr int D = 448, SK = 32768, DH = 256, WIN = 4096, FF = 1792;
constexpr int NQ = 4096;
constexpr int NWG = 256, NTHR = 1024;
constexpr int CONS0 = 224, NCONS = 14;
constexpr float EPS = 1e-5f;
constexpr float SCALE_LOG2E = 0.0625f * 1.4426950408889634f;
constexpr unsigned SENT = 0xFFFFFFFFu;
constexpr unsigned SPIN_CAP = 100000u;
constexpr unsigned LSPIN_CAP = 4000000u;

constexpr unsigned WS_E = 0;
constexpr unsigned WS_F = 4096;
constexpr unsigned WS_QG = 8192;
constexpr unsigned WS_HG = 147456;
constexpr unsigned WS_S2 = 262144;
constexpr unsigned WS_S3 = 1048576;
constexpr unsigned WS_BYTES = WS_S3 + NWG * 1024;

__device__ __forceinline__ float bf_lo(unsigned v) { return __uint_as_float(v << 16); }
__device__ __forceinline__ float bf_hi(unsigned v) { return __uint_as_float(v & 0xffff0000u); }
__device__ __forceinline__ float bf2f(u16 v) { return __uint_as_float((unsigned)v << 16); }
__device__ __forceinline__ unsigned cvtpk(float lo, float hi) {
  const bf16x2 v = {(__bf16)lo, (__bf16)hi};
  return __builtin_bit_cast(unsigned, v);
}
__device__ __forceinline__ float rbf(float x) { return bf_lo(cvtpk(x, x)); }

template <int CTRL> __device__ __forceinline__ float dpp(float x) {
  return __int_as_float(__builtin_amdgcn_mov_dpp(__float_as_int(x), CTRL, 0xf, 0xf, true));
}
constexpr int ROR1 = 0x121, ROR2 = 0x122, ROR4 = 0x124, ROR8 = 0x128, QXOR1 = 0xB1, QXOR2 = 0x4E, HMIRROR = 0x141;
__device__ __forceinline__ float row_sum(float x) {
  x += dpp<ROR8>(x); x += dpp<ROR4>(x); x += dpp<ROR2>(x); x += dpp<ROR1>(x); return x;
}
__device__ __forceinline__ float fold32(float a, float b) {
  auto r = __builtin_amdgcn_permlane32_swap(__float_as_uint(a), __float_as_uint(b), false, false);
  return __uint_as_float(r[0]) + __uint_as_float(r[1]);
}
__device__ __forceinline__ float fold16(float a, float b) {
  auto r = __builtin_amdgcn_permlane16_swap(__float_as_uint(a), __float_as_uint(b), false, false);
  return __uint_as_float(r[0]) + __uint_as_float(r[1]);
}
__device__ __forceinline__ float row_max(float x) {
  x = fmaxf(x, dpp<ROR8>(x)); x = fmaxf(x, dpp<ROR4>(x)); x = fmaxf(x, dpp<ROR2>(x)); x = fmaxf(x, dpp<ROR1>(x)); return x;
}
__device__ __forceinline__ float xrow_sum(float x) { x = fold16(x, x); return fold32(x, x); }
__device__ __forceinline__ float xrow_max(float x) {
  auto s = __builtin_amdgcn_permlane16_swap(__float_as_uint(x), __float_as_uint(x), false, false);
  x = fmaxf(__uint_as_float(s[0]), __uint_as_float(s[1]));
  auto t = __builtin_amdgcn_permlane32_swap(__float_as_uint(x), __float_as_uint(x), false, false);
  return fmaxf(__uint_as_float(t[0]), __uint_as_float(t[1]));
}
__device__ __forceinline__ float wave_sum(float x) { return xrow_sum(row_sum(x)); }

__device__ __forceinline__ u32x4 ld16(const u16* p) { return *reinterpret_cast<const u32x4*>(p); }
__device__ __forceinline__ __amdgpu_buffer_rsrc_t mkrs(const void* p, unsigned bytes) {
  return __builtin_amdgcn_make_buffer_rsrc(const_cast<void*>(p), 0, bytes, 0x00020000);
}
__device__ __forceinline__ u32x4 ldnt(__amdgpu_buffer_rsrc_t r, unsigned elem) { return __builtin_amdgcn_raw_buffer_load_b128(r, elem * 2u, 0, 2); }
__device__ __forceinline__ f32x4 mfma(u32x4 a, u32x4 b, f32x4 c) {
  return __builtin_amdgcn_mfma_f32_16x16x32_bf16(__builtin_bit_cast(bf16x8, a), __builtin_bit_cast(bf16x8, b), c, 0, 0, 0);
}
__device__ __forceinline__ u32x4 place(unsigned v, u32x4 pm) { return u32x4{v & pm[0], v & pm[1], v & pm[2], v & pm[3]}; }
__device__ __forceinline__ unsigned san(float v) {
  const unsigned b = __float_as_uint(v);
  return b == SENT ? 0x7fc00000u : b;
}
__device__ __forceinline__ unsigned sanu(unsigned b) { return b == SENT ? 0x7fc07fc0u : b; }
__device__ __forceinline__ u32x4 san4(f32x4 v) { return u32x4{san(v[0]), san(v[1]), san(v[2]), san(v[3])}; }
__device__ __forceinline__ unsigned umax4(u32x4 v) { return max(max(v[0], v[1]), max(v[2], v[3])); }
__device__ __forceinline__ float asf(unsigned v) { return __uint_as_float(v); }

enum { FL_QP = 0, FL_QPUB, FL_Q, FL_MID, FL_HPUB, FL_ST, FL_ISS, FL_PART, FL_H, FL_F, FL_S3PUB, FL_FIN, FL_N };

struct Smem {
  float obuf[8][512];
  float qpart[14][64];
  unsigned qs[256];
  float wmax[2][8];
  float wsum[2][8];
  unsigned hd[8];
  float fin[4][32];
  u64 flags[16];
  u64 eall[8];
};

__device__ __forceinline__ void lds_signal(u64* f, int idx) {
  asm volatile("" ::: "memory");
  __hip_atomic_store(reinterpret_cast<unsigned char*>(f) + idx, (unsigned char)1, RLX_WG);
}
constexpr u64 ALL8 = 0x0101010101010101ull, ALL7 = 0x0001010101010101ull, ALL4 = 0x01010101ull, ALL1 = 1ull;
template <int SLEEP>
__device__ __forceinline__ void lds_wait(u64* f, u64 mask) {
#pragma nounroll
  for (unsigned spins = 0; spins < LSPIN_CAP; ++spins) {
    if (!__any((__hip_atomic_load(f, RLX_WG) & mask) != mask)) break;
    if (SLEEP) __builtin_amdgcn_s_sleep(SLEEP);
  }
  asm volatile("" ::: "memory");
}
template <int N> __device__ __forceinline__ void waitvm() { asm volatile("s_waitcnt vmcnt(%0)" :: "i"(N) : "memory"); }
__device__ __forceinline__ void wg_barrier() {
  __builtin_amdgcn_sched_barrier(0);
  asm volatile("s_waitcnt lgkmcnt(0)" ::: "memory");
  __builtin_amdgcn_s_barrier();
  asm volatile("" ::: "memory");
  __builtin_amdgcn_sched_barrier(0);
}
#define SWIN 3
#define SPRE 3
#define STHR 16
#define SMID 9
#define HDELAY 1
#define HGATE ALL1
#define WDELAY 14
#define DDELAY 10
#define QDELAY 12

__device__ __forceinline__ float pick8(u32x4 xv, int src, int lane) {
  const unsigned d0 = __builtin_amdgcn_readlane(xv[0], src), d1 = __builtin_amdgcn_readlane(xv[1], src);
  const unsigned d2 = __builtin_amdgcn_readlane(xv[2], src), d3 = __builtin_amdgcn_readlane(xv[3], src);
  const unsigned lo = (lane & 16) ? d1 : d0, hi = (lane & 16) ? d3 : d2;
  const unsigned d = (lane & 32) ? hi : lo;
  return (lane & 8) ? bf_hi(d) : bf_lo(d);
}

__device__ __forceinline__ void ln_stats(const float (&v)[8], int lane, float& mean, float& rstd) {
  float sum = ((v[0] + v[1]) + (v[2] + v[3])) + ((v[4] + v[5]) + (v[6] + v[7]));
  if (lane >= 56) sum = 0.f;
  mean = wave_sum(sum) * (1.f / D);
  float sq = 0.f;
#pragma unroll
  for (int i = 0; i < 8; ++i) { const float d = v[i] - mean; sq = fmaf(d, d, sq); }
  if (lane >= 56) sq = 0.f;
  rstd = __builtin_amdgcn_rsqf(wave_sum(sq) * (1.f / D) + EPS);
}

__global__ void __launch_bounds__(NTHR, 4)
fused_kernel(const u16* __restrict__ x, const u16* __restrict__ kc, const u16* __restrict__ vc,
             const u16* __restrict__ wq, const u16* __restrict__ wo, const u16* __restrict__ w1,
             const u16* __restrict__ b1, const u16* __restrict__ w2, const u16* __restrict__ b2,
             u16* __restrict__ out, unsigned char* ws) {
  __shared__ __attribute__((aligned(16))) Smem sm;
  const int tid = threadIdx.x, lane = tid & 63;
  const int wave = __builtin_amdgcn_readfirstlane(tid >> 6);
  const int b = blockIdx.x, h = b & 7, s = b >> 3;
  const int g = lane >> 4, n = lane & 15;
  if (tid >= 512 && tid < 528) sm.flags[tid - 512] = 0ull;
  TR_INIT();
  TR(0);
  asm volatile("" :: "s"(x), "s"(kc), "s"(vc), "s"(wq), "s"(wo), "s"(w1), "s"(b1), "s"(w2), "s"(b2), "s"(out), "s"(ws));

  u32x4 pm;
  {
    const unsigned half = (lane & 1) ? 0xffff0000u : 0x0000ffffu;
#pragma unroll
    for (int i = 0; i < 4; ++i) pm[i] = (((lane & 7) >> 1) == i) ? half : 0u;
  }

  const int aw = wave;
  const __amdgpu_buffer_rsrc_t rs = __builtin_amdgcn_make_buffer_rsrc(ws, 0, WS_BYTES, 0x00020000);
  const unsigned rec = (unsigned)(h * 32 + s);
  const bool has_mlp = s < 28;
  const bool is_cons = b >= CONS0 && b < CONS0 + NCONS;
  unsigned yd[7];
  if (aw < 7) {
    const u32x4 xv = ld16(x + 8 * (lane < 56 ? lane : 0));
    const int row = (s >> 3) * 112 + aw * 16 + (lane >> 3);
    const __amdgpu_buffer_rsrc_t rq = mkrs(wq, D * NQ * 2);
    const unsigned wqo = (unsigned)(row * NQ + h * 512 + (s & 7) * 64 + (lane & 7) * 8);
    __builtin_amdgcn_sched_barrier(0);
    const u32x4 wq0 = ldnt(rq, wqo), wq1 = ldnt(rq, wqo + 8 * NQ);
    TR(1);
    wg_barrier();
    TR(2);
    float xf[8], mean, rstd;
#pragma unroll
    for (int i = 0; i < 4; ++i) { xf[2 * i] = bf_lo(xv[i]); xf[2 * i + 1] = bf_hi(xv[i]); }
    ln_stats(xf, lane, mean, rstd);
    const int src = (s >> 3) * 14 + aw * 2;
    const float n0 = (pick8(xv, src, lane) - mean) * rstd, n1 = (pick8(xv, src + 1, lane) - mean) * rstd;
    f32x4 a = mfma(place(cvtpk(n0, n0), pm), wq0, f32x4{0.f, 0.f, 0.f, 0.f});
    a = mfma(place(cvtpk(n1, n1), pm), wq1, a);
    if ((n >> 3) == (g >> 1)) *reinterpret_cast<f32x4*>(&sm.qpart[2 * aw + (g >> 1)][8 * (n & 7) + 4 * (g & 1)]) = a;
    lds_signal(&sm.flags[FL_QP], aw);
    TR(3);
    if (aw == 4 && has_mlp) {
      float mean2, rstd2;
#pragma unroll
      for (int i = 0; i < 8; ++i) xf[i] = rbf((xf[i] - mean) * rstd);
      ln_stats(xf, lane, mean2, rstd2);
#pragma unroll
      for (int i = 0; i < 7; ++i) {
        const float yv = (rbf((pick8(xv, h * 7 + i, lane) - mean) * rstd) - mean2) * rstd2;
        yd[i] = cvtpk(yv, yv);
      }
    }
  } else if (aw == 7) {
    const u64 e0 = __hip_atomic_load((gu64*)(ws + WS_E + (lane & 7) * 256), RLX_AGENT);
    const u32x4 sent4 = {SENT, SENT, SENT, SENT};
    __builtin_amdgcn_raw_buffer_store_b128(sent4, rs, WS_S2 + rec * 2048u + lane * 16u, 0, 16);
    __builtin_amdgcn_raw_buffer_store_b128(sent4, rs, lane < 56 ? WS_S3 + (unsigned)b * 1024u + lane * 16u : WS_S2 + rec * 2048u + 1024u + (lane - 56) * 16u, 0, 16);
    TR(1);
    wg_barrier();
    TR(2);
    const u64 e1 = e0 + 1ull;
    sm.eall[lane & 7] = e1;
    const u64 tagw = sm.eall[h] << 24;
    TR(3);
    lds_wait<0>(&sm.flags[FL_QP], ALL7);
    float t[14];
#pragma unroll
    for (int i = 0; i < 14; ++i) t[i] = sm.qpart[i][lane];
    const float qv = (((t[0] + t[1]) + (t[2] + t[3])) + ((t[4] + t[5]) + (t[6] + t[7]))) + (((t[8] + t[9]) + (t[10] + t[11])) + (t[12] + t[13]));
    __hip_atomic_store((gu64*)(ws + WS_QG) + ((h * 4 + (s >> 3)) * 512 + (s & 7) * 64 + lane), tagw | (u64)(__float_as_uint(qv) >> 8), RLX_AGENT);
    lds_signal(&sm.flags[FL_QPUB], 0);
    TR(13);
    waitvm<1>();
    if (lane == h) __hip_atomic_store((gu64*)(ws + WS_F) + rec, e1, RLX_AGENT);
  }

  if (wave >= 8) {
    const int sw = wave & 7;
    const size_t kvbase = ((size_t)h * SK + (SK - WIN) + s * 128 + sw * 16) * DH;
    const __amdgpu_buffer_rsrc_t rk = mkrs(kc + kvbase, (16 * DH * 2)), rv = mkrs(vc + kvbase, (16 * DH * 2));
    const unsigned ko = (unsigned)((n & 12) * DH + ((n & 3) + 4 * g) * 8), vo = (unsigned)(4 * g * DH + n * 8);
    u32x4 kv[16];
#pragma unroll
    for (int i = 0; i < 16; ++i) {
      if (i == SPRE) { TR(1); wg_barrier(); TR(2); }
      if (i == SMID + 1) lds_signal(&sm.flags[FL_MID], sw);
      if (i >= SWIN && i < STHR) waitvm<SWIN - 1>();
      kv[i] = i < 8 ? ldnt(rk, ko + (i >> 1) * DH + (i & 1) * 128) : ldnt(rv, vo + ((i - 8) >> 1) * DH + (i & 1) * 128);
    }
    __builtin_amdgcn_sched_barrier(0);
    lds_signal(&sm.flags[FL_ISS], sw);
    TR(3);
    lds_wait<0>(&sm.flags[FL_Q], ALL4);
    TR(4);
    const u32x4 qf0 = *reinterpret_cast<const u32x4*>(&sm.qs[(n >> 3) * 128 + ((n & 3) + 4 * g) * 4]);
    const u32x4 qf1 = *reinterpret_cast<const u32x4*>(&sm.qs[(n >> 3) * 128 + ((n & 3) + 4 * g) * 4 + 64]);
    float sv[4];
#pragma unroll
    for (int t = 0; t < 4; ++t) {
      f32x4 d = mfma(kv[2 * t], qf0, f32x4{0.f, 0.f, 0.f, 0.f});
      d = mfma(kv[2 * t + 1], qf1, d);
      float v = (n & 2) ? ((n & 1) ? d[3] : d[2]) : ((n & 1) ? d[1] : d[0]);
      v += dpp<QXOR1>(v); v += dpp<QXOR2>(v);
      sv[t] = v * SCALE_LOG2E;
    }
    const float m = xrow_max(fmaxf(fmaxf(sv[0], sv[1]), fmaxf(sv[2], sv[3])));
    float p[4];
#pragma unroll
    for (int t = 0; t < 4; ++t) p[t] = __builtin_amdgcn_exp2f(sv[t] - m);
    const float l = xrow_sum((p[0] + p[1]) + (p[2] + p[3]));
    sm.wmax[n >> 3][sw] = m;
    sm.wsum[n >> 3][sw] = l;
    lds_signal(&sm.flags[FL_ST], sw);
    TR(5);
    u32x4 af[4];
#pragma unroll
    for (int t = 0; t < 4; ++t) af[t] = place(cvtpk(p[t], p[t]), pm);
    __builtin_amdgcn_sched_barrier(0);
    f32x4 o0 = {0.f, 0.f, 0.f, 0.f}, o1 = {0.f, 0.f, 0.f, 0.f};
#pragma unroll
    for (int t = 0; t < 4; ++t) { o0 = mfma(af[t], kv[8 + 2 * t], o0); o1 = mfma(af[t], kv[9 + 2 * t], o1); }
    float* ob = &sm.obuf[sw][(g >> 1) * 256 + 8 * n + 4 * (g & 1)];
    *reinterpret_cast<f32x4*>(ob) = o0;
    *reinterpret_cast<f32x4*>(ob + 128) = o1;
    lds_signal(&sm.flags[FL_PART], sw);
    TR(7);
  }

  if (aw < 4) {
    {
      lds_wait<1>(&sm.flags[FL_QPUB], ALL1);
      __builtin_amdgcn_s_sleep(QDELAY);
      const u64 epoch1 = sm.eall[h];
      const unsigned tag_lo = (unsigned)epoch1 & 0xffu, tag_hi = (unsigned)(epoch1 >> 8);
      const unsigned qoff = WS_QG + (unsigned)(h * 2048 + aw * 128 + 2 * lane) * 8u;
      u32x4 gq[4];
#pragma nounroll
      for (unsigned spins = 0; spins < SPIN_CAP; ++spins) {
        asm volatile("" ::: "memory");
        unsigned bad = 0u;
#pragma unroll
        for (int i = 0; i < 4; ++i) gq[i] = __builtin_amdgcn_raw_buffer_load_b128(rs, qoff + i * 4096u, 0, 16);
#pragma unroll
        for (int i = 0; i < 4; ++i) bad |= ((gq[i][0] >> 24) ^ tag_lo) | (gq[i][1] ^ tag_hi) | ((gq[i][2] >> 24) ^ tag_lo) | (gq[i][3] ^ tag_hi);
        if (!__any(bad != 0u)) break;
        __builtin_amdgcn_s_sleep(12);
      }
      const float q0 = (asf(gq[0][0] << 8) + asf(gq[1][0] << 8)) + (asf(gq[2][0] << 8) + asf(gq[3][0] << 8));
      const float q1 = (asf(gq[0][2] << 8) + asf(gq[1][2] << 8)) + (asf(gq[2][2] << 8) + asf(gq[3][2] << 8));
      sm.qs[aw * 64 + lane] = cvtpk(q0, q1);
      lds_signal(&sm.flags[FL_Q], aw);
      TR(4);
    }
    u32x4 woreg[4], w2reg[2];
    const int colw = aw * 128 + n * 8;
    const bool colok = colw < D;
    lds_wait<2>(&sm.flags[FL_F], ALL1);
    lds_wait<1>(&sm.flags[FL_ST], ALL8);
    TR(9);
    {
      const int colc = colok ? colw : colw - 64;
      const __amdgpu_buffer_rsrc_t ro = mkrs(wo + (size_t)(h * 512 + s * 16) * D, (16 * D * 2));
#pragma unroll
      for (int t = 0; t < 4; ++t) woreg[t] = ldnt(ro, (unsigned)((8 * (t >> 1) + 2 * g + (t & 1)) * D + colc));
      if (has_mlp) {
        const __amdgpu_buffer_rsrc_t r2 = mkrs(w2 + (size_t)(b * 8) * D, (8 * D * 2));
#pragma unroll
        for (int t = 0; t < 2; ++t) w2reg[t] = ldnt(r2, (unsigned)((2 * g + t) * D + colc));
      }
    }
    __builtin_amdgcn_sched_barrier(0);
    {
      const f32x4 ma = *reinterpret_cast<const f32x4*>(&sm.wmax[aw >> 1][0]);
      const f32x4 mb = *reinterpret_cast<const f32x4*>(&sm.wmax[aw >> 1][4]);
      const float M = fmaxf(fmaxf(fmaxf(ma[0], ma[1]), fmaxf(ma[2], ma[3])), fmaxf(fmaxf(mb[0], mb[1]), fmaxf(mb[2], mb[3])));
      float wt[8];
#pragma unroll
      for (int w = 0; w < 4; ++w) { wt[w] = __builtin_amdgcn_exp2f(ma[w] - M); wt[4 + w] = __builtin_amdgcn_exp2f(mb[w] - M); }
      if ((aw & 1) == 0) {
        const f32x4 la = *reinterpret_cast<const f32x4*>(&sm.wsum[aw >> 1][0]);
        const f32x4 lb = *reinterpret_cast<const f32x4*>(&sm.wsum[aw >> 1][4]);
        const float L = ((wt[0] * la[0] + wt[1] * la[1]) + (wt[2] * la[2] + wt[3] * la[3])) + ((wt[4] * lb[0] + wt[5] * lb[1]) + (wt[6] * lb[2] + wt[7] * lb[3]));
        if (lane == 0) __builtin_amdgcn_raw_buffer_store_b64(u32x2{san(M), san(L)}, rs, WS_S2 + rec * 2048u + 1024u + (aw >> 1) * 8u, 0, 16);
      }
      f32x2 pv[8];
#pragma nounroll
      for (unsigned spins = 0; spins < LSPIN_CAP; ++spins) {
        const u64 fl = __hip_atomic_load(&sm.flags[FL_PART], RLX_WG);
        asm volatile("" ::: "memory");
#pragma unroll
        for (int w = 0; w < 8; ++w) pv[w] = *reinterpret_cast<const f32x2*>(&sm.obuf[w][aw * 128 + 2 * lane]);
        asm volatile("" ::: "memory");
        if (!__any(fl != ALL8)) break;
      }
      TR(6);
      const f32x2 o = ((wt[0] * pv[0] + wt[1] * pv[1]) + (wt[2] * pv[2] + wt[3] * pv[3])) + ((wt[4] * pv[4] + wt[5] * pv[5]) + (wt[6] * pv[6] + wt[7] * pv[7]));
      __builtin_amdgcn_raw_buffer_store_b32(sanu(cvtpk(o[0], o[1])), rs, WS_S2 + rec * 2048u + (unsigned)(aw * 64 + lane) * 4u, 0, 16);
      TR(7);
    }
    f32x4 acc = {0.f, 0.f, 0.f, 0.f};
    __builtin_amdgcn_s_sleep(DDELAY);
    TR(10);
    {
      const unsigned doff = WS_S2 + (unsigned)(h * 32 + (lane >> 1)) * 2048u + s * 32u + (lane & 1) * 16u;
      const unsigned soff = WS_S2 + (unsigned)(h * 32 + (lane >> 1)) * 2048u + 1024u + (unsigned)(s >> 4) * 8u;
      u32x4 d = {SENT, SENT, SENT, SENT};
      u32x2 st = {SENT, SENT};
      bool need_h = has_mlp;
#pragma nounroll
      for (unsigned spins = 0; spins < SPIN_CAP; ++spins) {
        asm volatile("" ::: "memory");
        if (max(umax4(d), max(st[0], st[1])) == SENT) {
          d = __builtin_amdgcn_raw_buffer_load_b128(rs, doff, 0, 16);
          st = __builtin_amdgcn_raw_buffer_load_b64(rs, soff, 0, 16);
        }
        if (need_h && !__any(__hip_atomic_load(&sm.flags[FL_H], RLX_WG) == 0ull)) {
          asm volatile("" ::: "memory");
#pragma unroll
          for (int t = 0; t < 2; ++t) acc = mfma(place(sm.hd[2 * g + t], pm), w2reg[t], acc);
          need_h = false;
        }
        if (!need_h && !__any(max(umax4(d), max(st[0], st[1])) == SENT)) break;
      }
      TR(11);
      const float mp = asf(st[0]);
      float M = mp;
      M = fmaxf(M, dpp<ROR2>(M)); M = fmaxf(M, dpp<ROR4>(M)); M = fmaxf(M, dpp<ROR8>(M)); M = xrow_max(M);
      const float e = __builtin_amdgcn_exp2f(mp - M);
      float L = e * asf(st[1]);
      L += dpp<ROR2>(L); L += dpp<ROR4>(L); L += dpp<ROR8>(L); L = xrow_sum(L);
      const float inv = __builtin_amdgcn_rcpf(L);
      float c4[4], c2[2];
#pragma unroll
      for (int i = 0; i < 2; ++i) {
        c4[2 * i] = fold32(e * bf_lo(d[i]), e * bf_lo(d[i + 2]));
        c4[2 * i + 1] = fold32(e * bf_hi(d[i]), e * bf_hi(d[i + 2]));
      }
#pragma unroll
      for (int i = 0; i < 2; ++i) {
        float t = fold16(c4[i], c4[i + 2]);
        t += dpp<ROR2>(t); t += dpp<ROR4>(t); t += dpp<ROR8>(t);
        c2[i] = t;
      }
      const unsigned own = cvtpk(c2[0] * inv, c2[1] * inv);
      const unsigned oth = (unsigned)__builtin_amdgcn_mov_dpp((int)own, QXOR1, 0xf, 0xf, true);
      const unsigned lo = (n & 1) ? oth : own, hi = (n & 1) ? own : oth;
      f32x4 acc2 = mfma(place(__builtin_amdgcn_perm(lo, lo, 0x03020302u), pm), woreg[1], f32x4{0.f, 0.f, 0.f, 0.f});
      acc = mfma(place(__builtin_amdgcn_perm(lo, lo, 0x01000100u), pm), woreg[0], acc);
      acc2 = mfma(place(__builtin_amdgcn_perm(hi, hi, 0x03020302u), pm), woreg[3], acc2);
      acc = mfma(place(__builtin_amdgcn_perm(hi, hi, 0x01000100u), pm), woreg[2], acc);
      acc += acc2;
    }
    if (g < 2 && colok)
      __builtin_amdgcn_raw_buffer_store_b64(u32x2{sanu(cvtpk(acc[0], acc[1])), sanu(cvtpk(acc[2], acc[3]))}, rs,
                                            WS_S3 + (unsigned)b * 1024u + (unsigned)(colw + 4 * g) * 2u, 0, 16);
    if (is_cons) lds_signal(&sm.flags[FL_S3PUB], aw);
    TR(12);
  } else if (aw < 8) {
    if (aw == 4) {
      if (has_mlp) {
        const __amdgpu_buffer_rsrc_t r1 = mkrs(w1, D * FF * 2);
        const unsigned w1o = (unsigned)((h * 56 + (lane >> 3)) * FF + s * 64 + (lane & 7) * 8);
        lds_wait<2>(&sm.flags[FL_QPUB], ALL1);
        lds_wait<2>(&sm.flags[FL_MID], ALL8);
        TR(9);
        u32x4 w1r[7];
#pragma unroll
        for (int i = 0; i < 7; ++i) w1r[i] = ldnt(r1, w1o + i * 8 * FF);
        __builtin_amdgcn_sched_barrier(0);
        const u64 tagw = sm.eall[h] << 24;
        f32x4 a0 = {0.f, 0.f, 0.f, 0.f}, a1 = {0.f, 0.f, 0.f, 0.f};
#pragma unroll
        for (int i = 0; i < 7; ++i) { if (i & 1) a1 = mfma(place(yd[i], pm), w1r[i], a1); else a0 = mfma(place(yd[i], pm), w1r[i], a0); }
        a0 += a1;
        const bool use = (n >> 3) == (g >> 1);
        float t0 = fold32(use ? a0[0] : 0.f, use ? a0[2] : 0.f), t1 = fold32(use ? a0[1] : 0.f, use ? a0[3] : 0.f);
        t0 += dpp<ROR8>(t0); t1 += dpp<ROR8>(t1);
        const float hv = (n & 8) ? t1 : t0;
        const int col = 8 * (n & 7) + 4 * (g & 1) + (n >> 3) + 2 * (g >> 1);
        __hip_atomic_store((gu64*)(ws + WS_HG) + (b * 64 + col), tagw | (u64)(__float_as_uint(hv) >> 8), RLX_AGENT);
        lds_signal(&sm.flags[FL_HPUB], 0);
        TR(11);
      }
    } else if (aw == 5) {
      if (has_mlp) {
        const unsigned b1v = *reinterpret_cast<const unsigned*>(b1 + b * 8 + 2 * (lane & 3));
        const int pr = (lane >> 2) & 7;
        lds_wait<2>(&sm.flags[FL_HPUB], ALL1);
        lds_wait<2>(&sm.flags[FL_ISS], HGATE);
        const u64 ep = sm.eall[pr];
        const unsigned tag_lo = (unsigned)ep & 0xffu, tag_hi = (unsigned)(ep >> 8);
        const unsigned hoff = WS_HG + (unsigned)((s * 8 + pr) * 64 + h * 8 + 2 * (lane & 3)) * 8u;
        __builtin_amdgcn_s_sleep(HDELAY);
        TR(9);
        u32x4 gh;
#pragma nounroll
        for (unsigned spins = 0; spins < SPIN_CAP; ++spins) {
          asm volatile("" ::: "memory");
          gh = __builtin_amdgcn_raw_buffer_load_b128(rs, hoff, 0, 16);
          const unsigned bad = ((gh[0] >> 24) ^ tag_lo) | (gh[1] ^ tag_hi) | ((gh[2] >> 24) ^ tag_lo) | (gh[3] ^ tag_hi);
          if (!__any(bad != 0u)) break;
          __builtin_amdgcn_s_sleep(8);
        }
        float hv = fold16(asf(gh[0] << 8), asf(gh[2] << 8));
        hv += dpp<ROR4>(hv); hv += dpp<ROR8>(hv);
        hv += (g & 1) ? bf_hi(b1v) : bf_lo(b1v);
        const float u = 0.7978845608028654f * (hv + 0.044715f * hv * hv * hv);
        const float ge = hv / (1.f + __expf(-2.f * u));
        sm.hd[2 * (n & 3) + (g & 1)] = cvtpk(ge, ge);
        lds_signal(&sm.flags[FL_H], 0);
        TR(11);
      }
    } else if (aw == 6) {
      lds_wait<2>(&sm.flags[FL_Q], ALL4);
      const int ga = is_cons ? (lane >> 4) : h, gb = is_cons ? 4 + (lane >> 4) : h;
      const u64 ea = sm.eall[ga], eb = sm.eall[gb];
#pragma nounroll
      for (unsigned spins = 0; spins < SPIN_CAP; ++spins) {
        asm volatile("" ::: "memory");
        const u32x4 f0 = __builtin_amdgcn_raw_buffer_load_b128(rs, WS_F + (unsigned)ga * 256u + n * 16u, 0, 16);
        const u32x4 f1 = __builtin_amdgcn_raw_buffer_load_b128(rs, WS_F + (unsigned)gb * 256u + n * 16u, 0, 16);
        const unsigned bad = (f0[0] ^ (unsigned)ea) | (f0[1] ^ (unsigned)(ea >> 32)) | (f0[2] ^ (unsigned)ea) | (f0[3] ^ (unsigned)(ea >> 32)) |
                             (f1[0] ^ (unsigned)eb) | (f1[1] ^ (unsigned)(eb >> 32)) | (f1[2] ^ (unsigned)eb) | (f1[3] ^ (unsigned)(eb >> 32));
        if (!__any(bad != 0u)) break;
        __builtin_amdgcn_s_sleep(12);
      }
      lds_signal(&sm.flags[FL_F], 0);
      TR(9);
    }
  }
  if (is_cons && wave >= 8 && wave < 12) {
    {
      const int pw = wave & 3;
      const unsigned fo = (unsigned)((b - CONS0) * 32 + (lane & 31)) * 2u;
      const u16 fin_x = __builtin_amdgcn_raw_buffer_load_b16(mkrs(x, (D * 2)), fo, 0, 0);
      const u16 fin_b = __builtin_amdgcn_raw_buffer_load_b16(mkrs(b2, (D * 2)), fo, 0, 0);
      const u32x4 ones = place(0x3f803f80u, pm);
      lds_wait<1>(&sm.flags[FL_F], ALL1);
      const float fbase = bf2f(fin_x) + bf2f(fin_b);
      lds_wait<0>(&sm.flags[FL_S3PUB], ALL4);
      __builtin_amdgcn_s_sleep(DDELAY);
      u32x4 v[4];
#pragma unroll
      for (int i = 0; i < 4; ++i) v[i] = u32x4{SENT, SENT, SENT, SENT};
      const unsigned off = WS_S3 + (unsigned)(pw * 64 + (lane >> 2)) * 1024u + (unsigned)(b - CONS0) * 64u + (lane & 3) * 16u;
#pragma nounroll
      for (unsigned spins = 0; spins < SPIN_CAP; ++spins) {
        asm volatile("" ::: "memory");
#pragma unroll
        for (int i = 0; i < 4; ++i)
          if (umax4(v[i]) == SENT) v[i] = __builtin_amdgcn_raw_buffer_load_b128(rs, off + i * 16384u, 0, 16);
        if (!__any(max(max(umax4(v[0]), umax4(v[1])), max(umax4(v[2]), umax4(v[3]))) == SENT)) break;
      }
      TR(14);
      f32x4 r0 = mfma(ones, v[0], f32x4{0.f, 0.f, 0.f, 0.f}), r1 = mfma(ones, v[1], f32x4{0.f, 0.f, 0.f, 0.f});
      r0 = mfma(ones, v[2], r0); r1 = mfma(ones, v[3], r1);
      r0 += r1;
#pragma unroll
      for (int k = 0; k < 4; ++k) { float t = r0[k]; t += dpp<ROR4>(t); t += dpp<ROR8>(t); r0[k] = t; }
      *reinterpret_cast<f32x4*>(&sm.fin[pw][8 * (n & 3) + 4 * (g & 1)]) = r0;
      lds_signal(&sm.flags[FL_FIN], pw);
      if (pw == 2) {
        float f[4];
#pragma nounroll
        for (unsigned spins = 0; spins < LSPIN_CAP; ++spins) {
          const u64 fl = __hip_atomic_load(&sm.flags[FL_FIN], RLX_WG);
          asm volatile("" ::: "memory");
#pragma unroll
          for (int w = 0; w < 4; ++w) f[w] = sm.fin[w][lane & 31];
          asm volatile("" ::: "memory");
          if (!__any((fl & ALL4) != ALL4)) break;
        }
        const float t = ((f[0] + f[1]) + (f[2] + f[3])) + fbase;
        if (lane < 32) __builtin_amdgcn_raw_buffer_store_b16((u16)cvtpk(t, 0.f), mkrs(out, (D * 2)), fo, 0, 0);
        if (b == CONS0 && lane < 8) {
          const u64 e = sm.eall[lane];
          __builtin_amdgcn_raw_buffer_store_b64(u32x2{(unsigned)e, (unsigned)(e >> 32)}, rs, WS_E + lane * 256u, 0, 16);
        }
        TR(15);
      }
    }
  }
  TR_DUMP();
}

}

extern "C" void kernel_launch(void* const* d_in, const int* in_sizes, int n_in,
                              void* d_out, int out_size, void* d_ws, size_t ws_size,
                              hipStream_t stream) {
  (void)in_sizes; (void)n_in; (void)out_size; (void)ws_size;
  const u16* x = (const u16*)d_in[0];
  const u16* kc = (const u16*)d_in[1];
  const u16* vc = (const u16*)d_in[2];
  const u16* wq = (const u16*)d_in[3];
  const u16* wo = (const u16*)d_in[4];
  const u16* w1 = (const u16*)d_in[6];
  const u16* b1 = (const u16*)d_in[7];
  const u16* w2 = (const u16*)d_in[8];
  const u16* b2 = (const u16*)d_in[9];
  fused_kernel<<<dim3(NWG), dim3(NTHR), 0, stream>>>(x, kc, vc, wq, wo, w1, b1, w2, b2, (u16*)d_out,
                                                     (unsigned char*)d_ws);
}
```

```cpp
#include <hip/hip_runtime.h>
#include <hip/hip_bf16.h>
#include <stdint.h>

#define GAS __attribute__((address_space(1)))
typedef unsigned u32x4 __attribute__((ext_vector_type(4)));
typedef unsigned u32x2 __attribute__((ext_vector_type(2)));
typedef float f32x4 __attribute__((ext_vector_type(4)));
typedef float f32x2 __attribute__((ext_vector_type(2)));
typedef __bf16 bf16x8 __attribute__((ext_vector_type(8)));
typedef __bf16 bf16x2 __attribute__((ext_vector_type(2)));
typedef unsigned short u16;
typedef unsigned long long u64;
typedef GAS u64 gu64;
#define RLX_AGENT __ATOMIC_RELAXED, __HIP_MEMORY_SCOPE_AGENT
#define RLX_WG __ATOMIC_RELAXED, __HIP_MEMORY_SCOPE_WORKGROUP

namespace {

constexpr int D = 448, SK = 32768, DH = 256, WIN = 4096, FF = 1792;
constexpr int NQ = 4096;
constexpr int NWG = 256, NTHR = 1024;
constexpr int CONS0 = 224, NCONS = 14;
constexpr float EPS = 1e-5f;
constexpr float SCALE_LOG2E = 0.0625f * 1.4426950408889634f;
constexpr unsigned SENT = 0xFFFFFFFFu;
constexpr unsigned SPIN_CAP = 100000u;
constexpr unsigned LSPIN_CAP = 4000000u;

constexpr unsigned WS_E = 0;
constexpr unsigned WS_F = 4096;
constexpr unsigned WS_QG = 8192;
constexpr unsigned WS_HG = 147456;
constexpr unsigned WS_S2 = 262144;
constexpr unsigned WS_S3 = 1048576;
constexpr unsigned WS_BYTES = WS_S3 + NWG * 1024;

__device__ __forceinline__ float bf_lo(unsigned v) { return __uint_as_float(v << 16); }
__device__ __forceinline__ float bf_hi(unsigned v) { return __uint_as_float(v & 0xffff0000u); }
__device__ __forceinline__ float bf2f(u16 v) { return __uint_as_float((unsigned)v << 16); }
__device__ __forceinline__ unsigned cvtpk(float lo, float hi) {
  const bf16x2 v = {(__bf16)lo, (__bf16)hi};
  return __builtin_bit_cast(unsigned, v);
}
__device__ __forceinline__ float rbf(float x) { return bf_lo(cvtpk(x, x)); }

template <int CTRL> __device__ __forceinline__ float dpp(float x) {
  return __int_as_float(__builtin_amdgcn_mov_dpp(__float_as_int(x), CTRL, 0xf, 0xf, true));
}
constexpr int ROR1 = 0x121, ROR2 = 0x122, ROR4 = 0x124, ROR8 = 0x128, QXOR1 = 0xB1, QXOR2 = 0x4E;
__device__ __forceinline__ float row_sum(float x) {
  x += dpp<ROR8>(x); x += dpp<ROR4>(x); x += dpp<ROR2>(x); x += dpp<ROR1>(x); return x;
}
__device__ __forceinline__ float fold32(float a, float b) {
  auto r = __builtin_amdgcn_permlane32_swap(__float_as_uint(a), __float_as_uint(b), false, false);
  return __uint_as_float(r[0]) + __uint_as_float(r[1]);
}
__device__ __forceinline__ float fold16(float a, float b) {
  auto r = __builtin_amdgcn_permlane16_swap(__float_as_uint(a), __float_as_uint(b), false, false);
  return __uint_as_float(r[0]) + __uint_as_float(r[1]);
}
__device__ __forceinline__ float xrow_sum(float x) { x = fold16(x, x); return fold32(x, x); }
__device__ __forceinline__ float xrow_max(float x) {
  auto s = __builtin_amdgcn_permlane16_swap(__float_as_uint(x), __float_as_uint(x), false, false);
  x = fmaxf(__uint_as_float(s[0]), __uint_as_float(s[1]));
  auto t = __builtin_amdgcn_permlane32_swap(__float_as_uint(x), __float_as_uint(x), false, false);
  return fmaxf(__uint_as_float(t[0]), __uint_as_float(t[1]));
}
__device__ __forceinline__ float wave_sum(float x) { return xrow_sum(row_sum(x)); }

__device__ __forceinline__ u32x4 ld16(const u16* p) { return *reinterpret_cast<const u32x4*>(p); }
__device__ __forceinline__ __amdgpu_buffer_rsrc_t mkrs(const void* p, unsigned bytes) {
  return __builtin_amdgcn_make_buffer_rsrc(const_cast<void*>(p), 0, bytes, 0x00020000);
}
__device__ __forceinline__ u32x4 ldnt(__amdgpu_buffer_rsrc_t r, unsigned elem) { return __builtin_amdgcn_raw_buffer_load_b128(r, elem * 2u, 0, 2); }
__device__ __forceinline__ f32x4 mfma(u32x4 a, u32x4 b, f32x4 c) {
  return __builtin_amdgcn_mfma_f32_16x16x32_bf16(__builtin_bit_cast(bf16x8, a), __builtin_bit_cast(bf16x8, b), c, 0, 0, 0);
}
__device__ __forceinline__ u32x4 place(unsigned v, u32x4 pm) { return u32x4{v & pm[0], v & pm[1], v & pm[2], v & pm[3]}; }
__device__ __forceinline__ unsigned san(float v) {
  const unsigned b = __float_as_uint(v);
  return b == SENT ? 0x7fc00000u : b;
}
__device__ __forceinline__ unsigned sanu(unsigned b) { return b == SENT ? 0x7fc07fc0u : b; }
__device__ __forceinline__ unsigned umax4(u32x4 v) { return max(max(v[0], v[1]), max(v[2], v[3])); }
__device__ __forceinline__ int fkey(int b) { return b ^ ((b >> 31) & 0x7fffffff); }
template <int CTRL> __device__ __forceinline__ int dppi(int x) { return __builtin_amdgcn_mov_dpp(x, CTRL, 0xf, 0xf, true); }
__device__ __forceinline__ float asf(unsigned v) { return __uint_as_float(v); }

enum { FL_QP = 0, FL_QPUB, FL_Q, FL_MID, FL_HPUB, FL_ST, FL_ISS, FL_PART, FL_H, FL_F, FL_S3PUB, FL_FIN, FL_N };

struct Smem {
  float obuf[8][512];
  float qpart[14][64];
  unsigned qs[256];
  float wmax[2][8];
  float wsum[2][8];
  unsigned hd[8];
  float fin[4][32];
  u64 flags[16];
  u64 eall[8];
};

__device__ __forceinline__ void lds_signal(u64* f, int idx) {
  asm volatile("" ::: "memory");
  __hip_atomic_store(reinterpret_cast<unsigned char*>(f) + idx, (unsigned char)1, RLX_WG);
}
constexpr u64 ALL8 = 0x0101010101010101ull, ALL7 = 0x0001010101010101ull, ALL4 = 0x01010101ull, ALL1 = 1ull;
template <int SLEEP>
__device__ __forceinline__ void lds_wait(u64* f, u64 mask) {
#pragma nounroll
  for (unsigned spins = 0; spins < LSPIN_CAP; ++spins) {
    if (!__any((__hip_atomic_load(f, RLX_WG) & mask) != mask)) break;
    if (SLEEP) __builtin_amdgcn_s_sleep(SLEEP);
  }
  asm volatile("" ::: "memory");
}
template <int N> __device__ __forceinline__ void waitvm() { asm volatile("s_waitcnt vmcnt(%0)" :: "i"(N) : "memory"); }
__device__ __forceinline__ void wg_barrier() {
  __builtin_amdgcn_sched_barrier(0);
  asm volatile("s_waitcnt lgkmcnt(0)" ::: "memory");
  __builtin_amdgcn_s_barrier();
  asm volatile("" ::: "memory");
  __builtin_amdgcn_sched_barrier(0);
}
constexpr int SPRE = 3;
constexpr int SWIN = 3;
constexpr int SWIN2 = 4;
constexpr int STHR = 10;
constexpr int SMID = 9;
constexpr int KS = 4;
constexpr int QDELAY = 12;
constexpr int DDELAY = 26;
constexpr int EDELAY = 16;

__device__ __forceinline__ float pick8(u32x4 xv, int src, int lane) {
  const unsigned d0 = __builtin_amdgcn_readlane(xv[0], src), d1 = __builtin_amdgcn_readlane(xv[1], src);
  const unsigned d2 = __builtin_amdgcn_readlane(xv[2], src), d3 = __builtin_amdgcn_readlane(xv[3], src);
  const unsigned lo = (lane & 16) ? d1 : d0, hi = (lane & 16) ? d3 : d2;
  const unsigned d = (lane & 32) ? hi : lo;
  return (lane & 8) ? bf_hi(d) : bf_lo(d);
}

__device__ __forceinline__ void ln_stats(const float (&v)[8], int lane, float& mean, float& rstd) {
  float sum = ((v[0] + v[1]) + (v[2] + v[3])) + ((v[4] + v[5]) + (v[6] + v[7]));
  if (lane >= 56) sum = 0.f;
  mean = wave_sum(sum) * (1.f / D);
  float sq = 0.f;
#pragma unroll
  for (int i = 0; i < 8; ++i) { const float d = v[i] - mean; sq = fmaf(d, d, sq); }
  if (lane >= 56) sq = 0.f;
  const float var = rbf(wave_sum(sq) * (1.f / D));
  rstd = __builtin_amdgcn_rcpf(rbf(__builtin_amdgcn_sqrtf(rbf(var + EPS))));
  mean = rbf(mean);
}
__device__ __forceinline__ float ln_apply(float v, float mean, float rstd) { return rbf(v - mean) * rstd; }

__global__ void __launch_bounds__(NTHR, 4)
fused_kernel(const u16* __restrict__ x, const u16* __restrict__ kc, const u16* __restrict__ vc,
             const u16* __restrict__ wq, const u16* __restrict__ wo, const u16* __restrict__ w1,
             const u16* __restrict__ b1, const u16* __restrict__ w2, const u16* __restrict__ b2,
             u16* __restrict__ out, unsigned char* ws) {
  __shared__ __attribute__((aligned(16))) Smem sm;
  const int tid = threadIdx.x, lane = tid & 63;
  const int wave = __builtin_amdgcn_readfirstlane(tid >> 6);
  const int b = blockIdx.x, h = b & 7, s = b >> 3;
  const int g = lane >> 4, n = lane & 15;
  asm volatile("" :: "s"(x), "s"(kc), "s"(vc), "s"(wq), "s"(wo), "s"(w1), "s"(b1), "s"(w2), "s"(b2), "s"(out), "s"(ws));
  const int aw = wave, sw = wave & 7;
  const unsigned rec = (unsigned)(h * 32 + s);
  const __amdgpu_buffer_rsrc_t rs = __builtin_amdgcn_make_buffer_rsrc(ws, 0, WS_BYTES, 0x00020000);
  const size_t kvbase = ((size_t)h * SK + (SK - WIN) + s * 128 + sw * 16) * DH;
  const __amdgpu_buffer_rsrc_t rk = mkrs(kc + kvbase, 16 * DH * 2);
  const unsigned ko = (unsigned)((n & 12) * DH + ((n & 3) + 4 * g) * 8);
  const int src = (s >> 3) * 14 + aw * 2;

  u32x4 kv[16], xv, wqr[2];
  u64 e0;
  if (wave >= 8) {
#pragma unroll
    for (int i = 0; i < SPRE; ++i) kv[i] = ldnt(rk, ko + (i >> 1) * DH + (i & 1) * 128);
    if (wave == 8 && lane < 16) sm.flags[lane] = 0ull;
  } else if (wave < 7) {
    xv = ld16(x + 8 * (lane < 56 ? lane : 0));
    const __amdgpu_buffer_rsrc_t rq = mkrs(wq, D * NQ * 2);
    const unsigned wqo = (unsigned)((src * 8 + (lane >> 3)) * NQ + h * 512 + (s & 7) * 64 + (lane & 7) * 8);
    __builtin_amdgcn_sched_barrier(0);
    wqr[0] = ldnt(rq, wqo);
    wqr[1] = ldnt(rq, wqo + 8 * NQ);
  } else {
    e0 = __hip_atomic_load((gu64*)(ws + WS_E + (lane & 7) * 256), RLX_AGENT);
    const u32x4 sent4 = {SENT, SENT, SENT, SENT};
    __builtin_amdgcn_raw_buffer_store_b128(sent4, rs, WS_S2 + rec * 2048u + lane * 16u, 0, 16);
    __builtin_amdgcn_raw_buffer_store_b128(sent4, rs, lane < 56 ? WS_S3 + (unsigned)b * 1024u + lane * 16u : WS_S2 + rec * 2048u + 1024u + ((lane - 56) >> 2) * 128u + (lane & 3) * 16u, 0, 16);
  }
  wg_barrier();

  u32x4 pm;
  {
    const unsigned half = (lane & 1) ? 0xffff0000u : 0x0000ffffu;
#pragma unroll
    for (int i = 0; i < 4; ++i) pm[i] = (((lane & 7) >> 1) == i) ? half : 0u;
  }
  const bool has_mlp = s < 28;
  const bool is_cons = b >= CONS0 && b < CONS0 + NCONS;
  unsigned yd[7];
  if (aw < 7) {
    float xf[8], mean, rstd;
#pragma unroll
    for (int i = 0; i < 4; ++i) { xf[2 * i] = bf_lo(xv[i]); xf[2 * i + 1] = bf_hi(xv[i]); }
    ln_stats(xf, lane, mean, rstd);
    {
      f32x4 a = {0.f, 0.f, 0.f, 0.f};
#pragma unroll
      for (int i = 0; i < 2; ++i) {
        const float nv = ln_apply(pick8(xv, src + i, lane), mean, rstd);
        a = mfma(place(cvtpk(nv, nv), pm), wqr[i], a);
      }
      if ((n >> 3) == (g >> 1)) *reinterpret_cast<f32x4*>(&sm.qpart[2 * aw + (g >> 1)][8 * (n & 7) + 4 * (g & 1)]) = a;
      lds_signal(&sm.flags[FL_QP], aw);
    }
    if (aw == 4 && has_mlp) {
      float mean2, rstd2;
#pragma unroll
      for (int i = 0; i < 8; ++i) xf[i] = rbf(ln_apply(xf[i], mean, rstd));
      ln_stats(xf, lane, mean2, rstd2);
#pragma unroll
      for (int i = 0; i < 7; ++i) {
        const float yv = ln_apply(rbf(ln_apply(pick8(xv, h * 7 + i, lane), mean, rstd)), mean2, rstd2);
        yd[i] = cvtpk(yv, yv);
      }
    }
  } else if (aw == 7) {
    asm volatile("" : "+v"(e0));
    const u64 e1 = e0 + 1ull;
    sm.eall[lane & 7] = e1;
    const u64 tagw = sm.eall[h] << 24;
    lds_wait<0>(&sm.flags[FL_QP], ALL7);
    float t[14];
#pragma unroll
    for (int i = 0; i < 14; ++i) t[i] = sm.qpart[i][lane];
    const float qv = (((t[0] + t[1]) + (t[2] + t[3])) + ((t[4] + t[5]) + (t[6] + t[7]))) + (((t[8] + t[9]) + (t[10] + t[11])) + (t[12] + t[13]));
    waitvm<0>();
    __hip_atomic_store((gu64*)(ws + WS_QG) + ((h * 4 + (s >> 3)) * 512 + (s & 7) * 64 + lane), tagw | (u64)(__float_as_uint(qv) >> 8), RLX_AGENT);
    lds_signal(&sm.flags[FL_QPUB], 0);
    if (lane == h) __hip_atomic_store((gu64*)(ws + WS_F) + rec, e1, RLX_AGENT);
  }

  if (wave >= 8) {
    const __amdgpu_buffer_rsrc_t rv = mkrs(vc + kvbase, 16 * DH * 2);
    const unsigned vo = (unsigned)(4 * g * DH + n * 8);
#pragma unroll
    for (int i = SPRE; i < 16; ++i) {
      if (i == SMID + 1) lds_signal(&sm.flags[FL_MID], sw);
      if (i >= SWIN && i < STHR) waitvm<SWIN - 1>();
      if (i >= STHR) waitvm<SWIN2 - 1>();
      kv[i] = i < 8 ? ldnt(rk, ko + (i >> 1) * DH + (i & 1) * 128) : ldnt(rv, vo + ((i - 8) >> 1) * DH + (i & 1) * 128);
    }
    __builtin_amdgcn_sched_barrier(0);
    lds_signal(&sm.flags[FL_ISS], sw);
    lds_wait<0>(&sm.flags[FL_Q], ALL4);
    const u32x4 qf0 = *reinterpret_cast<const u32x4*>(&sm.qs[(n >> 3) * 128 + ((n & 3) + 4 * g) * 4]);
    const u32x4 qf1 = *reinterpret_cast<const u32x4*>(&sm.qs[(n >> 3) * 128 + ((n & 3) + 4 * g) * 4 + 64]);
    float sv[4];
#pragma unroll
    for (int t = 0; t < 4; ++t) {
      f32x4 d = mfma(kv[2 * t], qf0, f32x4{0.f, 0.f, 0.f, 0.f});
      d = mfma(kv[2 * t + 1], qf1, d);
      float v = (n & 2) ? ((n & 1) ? d[3] : d[2]) : ((n & 1) ? d[1] : d[0]);
      v += dpp<QXOR1>(v); v += dpp<QXOR2>(v);
      sv[t] = rbf(v) * SCALE_LOG2E;
    }
    const float m = xrow_max(fmaxf(fmaxf(sv[0], sv[1]), fmaxf(sv[2], sv[3])));
    float p[4];
#pragma unroll
    for (int t = 0; t < 4; ++t) p[t] = __builtin_amdgcn_exp2f(sv[t] - m);
    const float l = xrow_sum((p[0] + p[1]) + (p[2] + p[3]));
    sm.wmax[n >> 3][sw] = m;
    sm.wsum[n >> 3][sw] = l;
    lds_signal(&sm.flags[FL_ST], sw);
    u32x4 af[4];
#pragma unroll
    for (int t = 0; t < 4; ++t) af[t] = place(cvtpk(p[t], p[t]), pm);
    __builtin_amdgcn_sched_barrier(0);
    f32x4 o0 = {0.f, 0.f, 0.f, 0.f}, o1 = {0.f, 0.f, 0.f, 0.f};
#pragma unroll
    for (int t = 0; t < 4; ++t) { o0 = mfma(af[t], kv[8 + 2 * t], o0); o1 = mfma(af[t], kv[9 + 2 * t], o1); }
    float* ob = &sm.obuf[sw][(g >> 1) * 256 + 8 * n + 4 * (g & 1)];
    *reinterpret_cast<f32x4*>(ob) = o0;
    *reinterpret_cast<f32x4*>(ob + 128) = o1;
    lds_signal(&sm.flags[FL_PART], sw);
  }

  if (aw < 4) {
    {
      lds_wait<1>(&sm.flags[FL_QPUB], ALL1);
      __builtin_amdgcn_s_sleep(QDELAY);
      const u64 epoch1 = sm.eall[h];
      const unsigned tag_lo = (unsigned)epoch1 & 0xffu, tag_hi = (unsigned)(epoch1 >> 8);
      const unsigned qoff = WS_QG + (unsigned)(h * 2048 + aw * 128 + 2 * lane) * 8u;
      u32x4 gq[KS];
#pragma nounroll
      for (unsigned spins = 0; spins < SPIN_CAP; ++spins) {
        asm volatile("" ::: "memory");
        unsigned bad = 0u;
#pragma unroll
        for (int i = 0; i < KS; ++i) gq[i] = __builtin_amdgcn_raw_buffer_load_b128(rs, qoff + i * 4096u, 0, 16);
#pragma unroll
        for (int i = 0; i < KS; ++i) bad |= ((gq[i][0] >> 24) ^ tag_lo) | (gq[i][1] ^ tag_hi) | ((gq[i][2] >> 24) ^ tag_lo) | (gq[i][3] ^ tag_hi);
        if (!__any(bad != 0u)) break;
        __builtin_amdgcn_s_sleep(12);
      }
      float q0 = 0.f, q1 = 0.f;
#pragma unroll
      for (int i = 0; i < KS; ++i) { q0 += asf(gq[i][0] << 8); q1 += asf(gq[i][2] << 8); }
      sm.qs[aw * 64 + lane] = cvtpk(q0, q1);
      lds_signal(&sm.flags[FL_Q], aw);
    }
    u32x4 woreg[4], w2reg[2];
    const int colw = aw * 128 + n * 8;
    const bool colok = colw < D;
    lds_wait<1>(&sm.flags[FL_ST], ALL8);
#define LOAD_WO() do { \
      const int colc = colok ? colw : colw - 64;          \
      const __amdgpu_buffer_rsrc_t ro = mkrs(wo + (size_t)(h * 512 + s * 16) * D, 16 * D * 2); \
      _Pragma("unroll") for (int t = 0; t < 4; ++t) woreg[t] = ldnt(ro, (unsigned)((8 * (t >> 1) + 2 * g + (t & 1)) * D + colc)); \
      if (has_mlp) { \
        const __amdgpu_buffer_rsrc_t r2 = mkrs(w2 + (size_t)(b * 8) * D, 8 * D * 2); \
        _Pragma("unroll") for (int t = 0; t < 2; ++t) w2reg[t] = ldnt(r2, (unsigned)((2 * g + t) * D + colc)); \
      } \
      __builtin_amdgcn_sched_barrier(0); } while (0)
    {
      const f32x4 ma = *reinterpret_cast<const f32x4*>(&sm.wmax[aw >> 1][0]);
      const f32x4 mb = *reinterpret_cast<const f32x4*>(&sm.wmax[aw >> 1][4]);
      const float M = fmaxf(fmaxf(fmaxf(ma[0], ma[1]), fmaxf(ma[2], ma[3])), fmaxf(fmaxf(mb[0], mb[1]), fmaxf(mb[2], mb[3])));
      float wt[8];
#pragma unroll
      for (int w = 0; w < 4; ++w) { wt[w] = __builtin_amdgcn_exp2f(ma[w] - M); wt[4 + w] = __builtin_amdgcn_exp2f(mb[w] - M); }
      if ((aw & 1) == 0) {
        const f32x4 la = *reinterpret_cast<const f32x4*>(&sm.wsum[aw >> 1][0]);
        const f32x4 lb = *reinterpret_cast<const f32x4*>(&sm.wsum[aw >> 1][4]);
        const float L = ((wt[0] * la[0] + wt[1] * la[1]) + (wt[2] * la[2] + wt[3] * la[3])) + ((wt[4] * lb[0] + wt[5] * lb[1]) + (wt[6] * lb[2] + wt[7] * lb[3]));
        const unsigned mk = (unsigned)fkey(__float_as_int(M + 0.f));
        if (lane == 0) __builtin_amdgcn_raw_buffer_store_b64(u32x2{mk, san(L)}, rs, WS_S2 + rec * 2048u + 1024u + (aw >> 1) * 128u, 0, 16);
      }
      f32x2 pv[8];
#pragma nounroll
      for (unsigned spins = 0; spins < LSPIN_CAP; ++spins) {
        const u64 fl = __hip_atomic_load(&sm.flags[FL_PART], RLX_WG);
        asm volatile("" ::: "memory");
#pragma unroll
        for (int w = 0; w < 8; ++w) pv[w] = *reinterpret_cast<const f32x2*>(&sm.obuf[w][aw * 128 + 2 * lane]);
        asm volatile("" ::: "memory");
        if (!__any(fl != ALL8)) break;
      }
      const f32x2 o = ((wt[0] * pv[0] + wt[1] * pv[1]) + (wt[2] * pv[2] + wt[3] * pv[3])) + ((wt[4] * pv[4] + wt[5] * pv[5]) + (wt[6] * pv[6] + wt[7] * pv[7]));
      const int ow = (int)sanu(cvtpk(o[0], o[1]));
      const u32x4 o4 = {(unsigned)ow, (unsigned)dppi<0x55>(ow), (unsigned)dppi<0xAA>(ow), (unsigned)dppi<0xFF>(ow)};
      if ((lane & 3) == 0) __builtin_amdgcn_raw_buffer_store_b128(o4, rs, WS_S2 + rec * 2048u + (unsigned)(aw * 64 + lane) * 4u, 0, 16);

    }
    LOAD_WO();
    f32x4 acc = {0.f, 0.f, 0.f, 0.f};
    __builtin_amdgcn_s_sleep(DDELAY);
    asm volatile("" ::: "memory");
    {
      const unsigned doff = WS_S2 + (unsigned)(h * 32 + (lane >> 1)) * 2048u + s * 32u + (lane & 1) * 16u;
      const unsigned soff = WS_S2 + (unsigned)(h * 32 + (lane >> 1)) * 2048u + 1024u + (unsigned)(s >> 4) * 128u;
      u32x4 d = __builtin_amdgcn_raw_buffer_load_b128(rs, doff, 0, 16);
      u32x2 st = __builtin_amdgcn_raw_buffer_load_b64(rs, soff, 0, 16);
      __builtin_amdgcn_sched_barrier(0);
      bool need_h = has_mlp;
      if (need_h && !__any(__hip_atomic_load(&sm.flags[FL_H], RLX_WG) == 0ull)) {
        asm volatile("" ::: "memory");
#pragma unroll
        for (int t = 0; t < 2; ++t) acc = mfma(place(sm.hd[2 * g + t], pm), w2reg[t], acc);
        need_h = false;
      }
      if (__builtin_expect(__any(max(umax4(d), max(st[0], st[1])) == SENT), 0)) {
#pragma nounroll
        for (unsigned spins = 0; spins < SPIN_CAP; ++spins) {
          asm volatile("" ::: "memory");
          if (max(umax4(d), max(st[0], st[1])) == SENT) {
            d = __builtin_amdgcn_raw_buffer_load_b128(rs, doff, 0, 16);
            st = __builtin_amdgcn_raw_buffer_load_b64(rs, soff, 0, 16);
          }
          if (!__any(max(umax4(d), max(st[0], st[1])) == SENT)) break;
        }
      }
      int mk = (int)st[0];
      mk = max(mk, dppi<ROR2>(mk)); mk = max(mk, dppi<ROR4>(mk)); mk = max(mk, dppi<ROR8>(mk));
      { auto r = __builtin_amdgcn_permlane16_swap((unsigned)mk, (unsigned)mk, false, false); mk = max((int)r[0], (int)r[1]); }
      { auto r = __builtin_amdgcn_permlane32_swap((unsigned)mk, (unsigned)mk, false, false); mk = max((int)r[0], (int)r[1]); }
      const float e = __builtin_amdgcn_exp2f(__int_as_float(fkey((int)st[0])) - __int_as_float(fkey(mk)));
      float c4[4], c2[2];
#pragma unroll
      for (int i = 0; i < 2; ++i) {
        c4[2 * i] = fold32(e * bf_lo(d[i]), e * bf_lo(d[i + 2]));
        c4[2 * i + 1] = fold32(e * bf_hi(d[i]), e * bf_hi(d[i + 2]));
      }
      float L = e * asf(st[1]);
      L = fold32(L, L);
#pragma unroll
      for (int i = 0; i < 2; ++i) c2[i] = fold16(c4[i], c4[i + 2]);
      L = fold16(L, L);
      c2[0] += dpp<ROR2>(c2[0]); c2[1] += dpp<ROR2>(c2[1]); L += dpp<ROR2>(L);
      c2[0] += dpp<ROR4>(c2[0]); c2[1] += dpp<ROR4>(c2[1]); L += dpp<ROR4>(L);
      c2[0] += dpp<ROR8>(c2[0]); c2[1] += dpp<ROR8>(c2[1]); L += dpp<ROR8>(L);
      const unsigned own = cvtpk(c2[0], c2[1]);
      const unsigned oth = (unsigned)dppi<QXOR1>((int)own);
      const unsigned lo = (n & 1) ? oth : own, hi = (n & 1) ? own : oth;
      f32x4 a0 = mfma(place(__builtin_amdgcn_perm(lo, lo, 0x01000100u), pm), woreg[0], f32x4{0.f, 0.f, 0.f, 0.f});
      f32x4 a1 = mfma(place(__builtin_amdgcn_perm(lo, lo, 0x03020302u), pm), woreg[1], f32x4{0.f, 0.f, 0.f, 0.f});
      a0 = mfma(place(__builtin_amdgcn_perm(hi, hi, 0x01000100u), pm), woreg[2], a0);
      a1 = mfma(place(__builtin_amdgcn_perm(hi, hi, 0x03020302u), pm), woreg[3], a1);
      const float inv = __builtin_amdgcn_rcpf(L);
      if (__builtin_expect(need_h, 0)) {
        lds_wait<0>(&sm.flags[FL_H], ALL1);
#pragma unroll
        for (int t = 0; t < 2; ++t) acc = mfma(place(sm.hd[2 * g + t], pm), w2reg[t], acc);
      }
      acc += (a0 + a1) * inv;
    }
    {
      const unsigned w0 = sanu(cvtpk(acc[0], acc[1])), w1v = sanu(cvtpk(acc[2], acc[3]));
      auto r0 = __builtin_amdgcn_permlane16_swap(w0, w0, false, false);
      auto r1 = __builtin_amdgcn_permlane16_swap(w1v, w1v, false, false);
      if (g == 0 && colok)
        __builtin_amdgcn_raw_buffer_store_b128(u32x4{r0[0], r1[0], r0[1], r1[1]}, rs, WS_S3 + (unsigned)b * 1024u + (unsigned)colw * 2u, 0, 16);
    }

    if (is_cons) lds_signal(&sm.flags[FL_S3PUB], aw);
  } else if (aw < 8) {
    if (aw == 4) {
      if (has_mlp) {
        const __amdgpu_buffer_rsrc_t r1 = mkrs(w1, D * FF * 2);
        const unsigned w1o = (unsigned)((h * 56 + (lane >> 3)) * FF + s * 64 + (lane & 7) * 8);
        lds_wait<2>(&sm.flags[FL_QPUB], ALL1);
        lds_wait<2>(&sm.flags[FL_MID], ALL8);
        u32x4 w1r[7];
#pragma unroll
        for (int i = 0; i < 7; ++i) w1r[i] = ldnt(r1, w1o + i * 8 * FF);
        __builtin_amdgcn_sched_barrier(0);
        const u64 tagw = sm.eall[h] << 24;
        f32x4 a0 = {0.f, 0.f, 0.f, 0.f}, a1 = {0.f, 0.f, 0.f, 0.f};
#pragma unroll
        for (int i = 0; i < 7; ++i) { if (i & 1) a1 = mfma(place(yd[i], pm), w1r[i], a1); else a0 = mfma(place(yd[i], pm), w1r[i], a0); }
        a0 += a1;
        const bool use = (n >> 3) == (g >> 1);
        float t0 = fold32(use ? a0[0] : 0.f, use ? a0[2] : 0.f), t1 = fold32(use ? a0[1] : 0.f, use ? a0[3] : 0.f);
        t0 += dpp<ROR8>(t0); t1 += dpp<ROR8>(t1);
        const float hv = (n & 8) ? t1 : t0;
        const int col = 8 * (n & 7) + 4 * (g & 1) + (n >> 3) + 2 * (g >> 1);
        __hip_atomic_store((gu64*)(ws + WS_HG) + (b * 64 + col), tagw | (u64)(__float_as_uint(hv) >> 8), RLX_AGENT);
        lds_signal(&sm.flags[FL_HPUB], 0);
      }
    } else if (aw == 5) {
      if (has_mlp) {
        const unsigned b1v = *reinterpret_cast<const unsigned*>(b1 + b * 8 + 2 * (lane & 3));
        const int pr = (lane >> 2) & 7;
        lds_wait<2>(&sm.flags[FL_HPUB], ALL1);
        lds_wait<2>(&sm.flags[FL_ISS], ALL1);
        const u64 ep = sm.eall[pr];
        const unsigned tag_lo = (unsigned)ep & 0xffu, tag_hi = (unsigned)(ep >> 8);
        const unsigned hoff = WS_HG + (unsigned)((s * 8 + pr) * 64 + h * 8 + 2 * (lane & 3)) * 8u;
        u32x4 gh;
#pragma nounroll
        for (unsigned spins = 0; spins < SPIN_CAP; ++spins) {
          asm volatile("" ::: "memory");
          gh = __builtin_amdgcn_raw_buffer_load_b128(rs, hoff, 0, 16);
          const unsigned bad = ((gh[0] >> 24) ^ tag_lo) | (gh[1] ^ tag_hi) | ((gh[2] >> 24) ^ tag_lo) | (gh[3] ^ tag_hi);
          if (!__any(bad != 0u)) break;
          __builtin_amdgcn_s_sleep(8);
        }
        float hv = fold16(asf(gh[0] << 8), asf(gh[2] << 8));
        hv += dpp<ROR4>(hv); hv += dpp<ROR8>(hv);
        hv = rbf(rbf(hv) + ((g & 1) ? bf_hi(b1v) : bf_lo(b1v)));
        const float u = 0.7978845608028654f * (hv + 0.044715f * hv * hv * hv);
        const float ge = hv / (1.f + __expf(-2.f * u));
        sm.hd[2 * (n & 3) + (g & 1)] = cvtpk(ge, ge);
        lds_signal(&sm.flags[FL_H], 0);
      }
    } else if (aw == 6 && is_cons) {
      lds_wait<2>(&sm.flags[FL_Q], ALL4);
      const u64 ea = sm.eall[g], eb = sm.eall[4 + g];
#pragma nounroll
      for (unsigned spins = 0; spins < SPIN_CAP; ++spins) {
        asm volatile("" ::: "memory");
        const u32x4 f0 = __builtin_amdgcn_raw_buffer_load_b128(rs, WS_F + lane * 16u, 0, 16);
        const u32x4 f1 = __builtin_amdgcn_raw_buffer_load_b128(rs, WS_F + 1024u + lane * 16u, 0, 16);
        const unsigned bad = (f0[0] ^ (unsigned)ea) | (f0[1] ^ (unsigned)(ea >> 32)) | (f0[2] ^ (unsigned)ea) | (f0[3] ^ (unsigned)(ea >> 32)) |
                             (f1[0] ^ (unsigned)eb) | (f1[1] ^ (unsigned)(eb >> 32)) | (f1[2] ^ (unsigned)eb) | (f1[3] ^ (unsigned)(eb >> 32));
        if (!__any(bad != 0u)) break;
        __builtin_amdgcn_s_sleep(12);
      }
      lds_signal(&sm.flags[FL_F], 0);
    }
  }
  if (is_cons && wave >= 8 && wave < 12) {
    {
      const int pw = wave & 3;
      const unsigned fo = (unsigned)((b - CONS0) * 32 + (lane & 31)) * 2u;
      const u16 fin_x = __builtin_amdgcn_raw_buffer_load_b16(mkrs(x, (D * 2)), fo, 0, 0);
      const u16 fin_b = __builtin_amdgcn_raw_buffer_load_b16(mkrs(b2, (D * 2)), fo, 0, 0);
      const u32x4 ones = place(0x3f803f80u, pm);
      lds_wait<1>(&sm.flags[FL_F], ALL1);
      const float fbase = bf2f(fin_x) + bf2f(fin_b);
      lds_wait<0>(&sm.flags[FL_S3PUB], ALL4);
      __builtin_amdgcn_s_sleep(EDELAY);
      asm volatile("" ::: "memory");
      u32x4 v[4];
      const unsigned off = WS_S3 + (unsigned)(pw * 64 + (lane >> 2)) * 1024u + (unsigned)(b - CONS0) * 64u + (lane & 3) * 16u;
#pragma unroll
      for (int i = 0; i < 4; ++i) v[i] = __builtin_amdgcn_raw_buffer_load_b128(rs, off + i * 16384u, 0, 16);
      if (__any(max(max(umax4(v[0]), umax4(v[1])), max(umax4(v[2]), umax4(v[3]))) == SENT)) {
#pragma nounroll
        for (unsigned spins = 0; spins < SPIN_CAP; ++spins) {
          asm volatile("" ::: "memory");
#pragma unroll
          for (int i = 0; i < 4; ++i)
            if (umax4(v[i]) == SENT) v[i] = __builtin_amdgcn_raw_buffer_load_b128(rs, off + i * 16384u, 0, 16);
          if (!__any(max(max(umax4(v[0]), umax4(v[1])), max(umax4(v[2]), umax4(v[3]))) == SENT)) break;
        }
      }
      f32x4 r0 = mfma(ones, v[0], f32x4{0.f, 0.f, 0.f, 0.f}), r1 = mfma(ones, v[1], f32x4{0.f, 0.f, 0.f, 0.f});
      r0 = mfma(ones, v[2], r0); r1 = mfma(ones, v[3], r1);
      r0 += r1;
#pragma unroll
      for (int k = 0; k < 4; ++k) { float t = r0[k]; t += dpp<ROR4>(t); t += dpp<ROR8>(t); r0[k] = t; }
      *reinterpret_cast<f32x4*>(&sm.fin[pw][8 * (n & 3) + 4 * (g & 1)]) = r0;
      lds_signal(&sm.flags[FL_FIN], pw);
      if (pw == 2) {
        float f[4];
#pragma nounroll
        for (unsigned spins = 0; spins < LSPIN_CAP; ++spins) {
          const u64 fl = __hip_atomic_load(&sm.flags[FL_FIN], RLX_WG);
          asm volatile("" ::: "memory");
#pragma unroll
          for (int w = 0; w < 4; ++w) f[w] = sm.fin[w][lane & 31];
          asm volatile("" ::: "memory");
          if (!__any((fl & ALL4) != ALL4)) break;
        }
        const float t = ((f[0] + f[1]) + (f[2] + f[3])) + fbase;
        if (lane < 32) __builtin_amdgcn_raw_buffer_store_b16((u16)cvtpk(t, 0.f), mkrs(out, (D * 2)), fo, 0, 0);
        if (b == CONS0 && lane < 8) {
          const u64 e = sm.eall[lane];
          __builtin_amdgcn_raw_buffer_store_b64(u32x2{(unsigned)e, (unsigned)(e >> 32)}, rs, WS_E + lane * 256u, 0, 16);
        }
      }
    }
  }
}

}

extern "C" void kernel_launch(void* const* d_in, const int* in_sizes, int n_in,
                              void* d_out, int out_size, void* d_ws, size_t ws_size,
                              hipStream_t stream) {
  (void)in_sizes; (void)n_in; (void)out_size; (void)ws_size;
  const u16* x = (const u16*)d_in[0];
  const u16* kc = (const u16*)d_in[1];
  const u16* vc = (const u16*)d_in[2];
  const u16* wq = (const u16*)d_in[3];
  const u16* wo = (const u16*)d_in[4];
  const u16* w1 = (const u16*)d_in[6];
  const u16* b1 = (const u16*)d_in[7];
  const u16* w2 = (const u16*)d_in[8];
  const u16* b2 = (const u16*)d_in[9];
  fused_kernel<<<dim3(NWG), dim3(NTHR), 0, stream>>>(x, kc, vc, wq, wo, w1, b1, w2, b2, (u16*)d_out,
                                                     (unsigned char*)d_ws);
}
```
